```python
import torch

def _softmax(x, axis=-1):
    return torch.softmax(x, dim=axis)
def _gelu(x):
    return torch.nn.functional.gelu(x, approximate="tanh")
def _silu(x):
    return torch.nn.functional.silu(x)
def _sigmoid(x):
    return torch.sigmoid(x)
def _layernorm(x, axis=-1, eps=1e-5):
    m = x.mean(dim=axis, keepdim=True)
    v = x.var(dim=axis, keepdim=True, unbiased=False)
    return (x - m) / torch.sqrt(v + eps)
def _scatter_add(tgt, idx, src, axis=0):
    return tgt.clone().index_add_(axis, idx, src)
def _segment_sum(src, seg, n):
    out = torch.zeros((n,) + src.shape[1:], dtype=src.dtype)
    return out.index_add_(0, seg, src)
def _pad(x, pw):
    flat = tuple(v for pair in reversed(pw) for v in pair)
    return torch.nn.functional.pad(x, flat)

kernel_name = 'gated_mix__grouped_kv_mqa__geglu_ffn__prompt'

def setup_inputs():

    _w = (lambda a: (lambda t: t.to(torch.bfloat16) if t.is_floating_point() else t)(torch.as_tensor(a)))
    def _A_setup():
        B, S, D, H = 1, 2048, 640, 32
        Dh = D // H
        return {
            "x": _w((lambda *s: torch.randn(*s, dtype=torch.bfloat16) * 0.05)(B, S, D)),
            "wq": _w((lambda *s: torch.randn(*s, dtype=torch.bfloat16) * 0.8)(D, H * Dh)),
            "wk": _w((lambda *s: torch.randn(*s, dtype=torch.bfloat16) * 0.8)(D, Dh)),
            "wv": _w((lambda *s: torch.randn(*s, dtype=torch.bfloat16) * 0.05)(D, Dh)),
            "wo": _w((lambda *s: torch.randn(*s, dtype=torch.bfloat16) * 0.05)(H * Dh, D)),
        }
    _A_in = _A_setup()

    def _B_setup():
        B, S, D, F = 1, 2048, 640, 1728
        return {
            "x": _w((lambda *s: torch.randn(*s, dtype=torch.bfloat16) * 0.05)(B, S, D)),
            "w_gate": _w((lambda *s: torch.randn(*s, dtype=torch.bfloat16) * 0.05)(D, F)),
            "w_up": _w((lambda *s: torch.randn(*s, dtype=torch.bfloat16) * 0.05)(D, F)),
            "w_down": _w((lambda *s: torch.randn(*s, dtype=torch.bfloat16) * 0.05)(F, D)),
        }
    _B_in = _B_setup()

    return {
        "A_x": _A_in["x"],
        "A_wq": _A_in["wq"],
        "A_wk": _A_in["wk"],
        "A_wv": _A_in["wv"],
        "A_wo": _A_in["wo"],
        "B_x": _B_in["x"],
        "B_w_gate": _B_in["w_gate"],
        "B_w_up": _B_in["w_up"],
        "B_w_down": _B_in["w_down"],
        "gate_w": _w((lambda *s: torch.randn(*s, dtype=torch.bfloat16) * 0.05)(640) * 16.0),
    }

def reference(A_x, A_wq, A_wk, A_wv, A_wo, B_x, B_w_gate, B_w_up, B_w_down, gate_w):
    def _A_ref(x, wq, wk, wv, wo):
        B, S, D = x.shape
        H = 32
        Dh = D // H
        q = (lambda x, *d: x.permute(*d))((x @ wq).reshape(B, S, H, Dh), 0, 2, 1, 3)
        k = x @ wk
        v = x @ wv
        scores = torch.einsum("bhqd,bkd->bhqk", q, k) * (Dh ** -0.5)
        p = _softmax(scores, axis=-1)
        o = torch.einsum("bhqk,bkd->bhqd", p, v)
        return (lambda x, *d: x.permute(*d))(o, 0, 2, 1, 3).reshape(B, S, H * Dh) @ wo

    def _B_ref(x, w_gate, w_up, w_down):
        return (_gelu(x @ w_gate) * (x @ w_up)) @ w_down

    x = A_x
    g = _sigmoid((x * gate_w).sum(-1, keepdims=True))
    a = _A_ref(x, A_wq, A_wk, A_wv, A_wo)
    b = _B_ref(x, B_w_gate, B_w_up, B_w_down)
    return g * a + (1 - g) * b
```

```cpp
#include <hip/hip_runtime.h>
#include <hip/hip_bf16.h>
#include <stdint.h>

typedef short bf16x8 __attribute__((ext_vector_type(8)));
typedef short s16x4 __attribute__((ext_vector_type(4)));
typedef short v4i16_t __attribute__((ext_vector_type(4)));
typedef float f32x16 __attribute__((ext_vector_type(16)));
typedef float f32x4 __attribute__((ext_vector_type(4)));
typedef float f32x2 __attribute__((ext_vector_type(2)));
typedef __bf16 bf16x2_t __attribute__((ext_vector_type(2)));
typedef unsigned u32x4 __attribute__((ext_vector_type(4)));
typedef unsigned u32x2 __attribute__((ext_vector_type(2)));
#define LAS __attribute__((address_space(3)))
#define GAS __attribute__((address_space(1)))
#define DI __device__ __forceinline__
typedef LAS char* lds_ptr;

constexpr int S = 2048, D = 640, NH = 32, DH = 20, F = 1728;
constexpr int KVT = 3584;
constexpr size_t WS_KV = 0, WS_G = 229376, WS_H = 1 << 20, WS_O = WS_H + (size_t)64 * 108 * 1024, WS_END = WS_O + (size_t)64 * 40 * 1024;
constexpr float QSCALE = 0.22360679774997896f * 1.4426950408889634f;

#define MFMA16(a, b, c) __builtin_amdgcn_mfma_f32_32x32x16_bf16((a), (b), (c), 0, 0, 0)
#define MFMA8(a, b, c) __builtin_amdgcn_mfma_f32_32x32x8bf16_1k((a), (b), (c), 0, 0, 0)

DI unsigned cvtpk(float lo, float hi) { f32x2 v = {lo, hi}; bf16x2_t b = __builtin_convertvector(v, bf16x2_t); return __builtin_bit_cast(unsigned, b); }
DI float bf16r(float x) { return __uint_as_float(cvtpk(x, 0.f) << 16); }
DI void glds16(const void* g, unsigned lds_base) {
    unsigned sv; asm volatile("s_mov_b32 %0, m0\n\ts_mov_b32 m0, %2\n\ts_nop 0\n\tglobal_load_lds_dwordx4 %1, off\n\ts_mov_b32 m0, %0" : "=&s"(sv) : "v"(g), "s"(lds_base) : "memory"); }
DI s16x4 trr(lds_ptr p) { return __builtin_bit_cast(s16x4, __builtin_amdgcn_ds_read_tr16_b64_v4i16((LAS v4i16_t*)p)); }
DI bf16x8 tr2(lds_ptr p, int off2) { s16x4 lo = trr(p), hi = trr(p + off2); return __builtin_shufflevector(lo, hi, 0, 1, 2, 3, 4, 5, 6, 7); }
DI bf16x8 ldsv(lds_ptr p) { return *(LAS bf16x8*)p; }
#define WAITBAR(N) asm volatile("s_waitcnt vmcnt(" #N ") lgkmcnt(0)\n\ts_barrier" ::: "memory")
DI unsigned rfl(unsigned x) { return __builtin_amdgcn_readfirstlane(x); }

constexpr int PX = 0, PWQ = 8192, PWKV = 18432, PSTG = 21504, PGATE = 3 * PSTG;
template <bool DOQ, bool DOKV>
DI void proj_phase(const uint16_t* __restrict__ x, const uint16_t* __restrict__ wq, const uint16_t* __restrict__ wkv, const uint16_t* __restrict__ gatew,
                   int tt, int hg, int kvhalf, bool isV, char* smem, f32x16 (&accq)[2], f32x16& acckv) {
    const int tid = threadIdx.x, lane = tid & 63, w = rfl(tid >> 6), r = lane & 31, h = lane >> 5;
    const unsigned lds0 = (unsigned)(uintptr_t)smem;
    lds_ptr L = (lds_ptr)smem;
    const char* xs[2]; const char* ws_[3]; const char* ks[2];
#pragma unroll
    for (int i = 0; i < 2; ++i) { const int row = (2 * w + i) * 8 + (lane >> 3), c = (lane & 7) ^ ((row >> 1) & 7); xs[i] = (const char*)(x + (size_t)(tt * 64 + row) * D) + c * 16; }
    const int wq0 = (w < 2) ? 3 * w : 6 + 2 * (w - 2), nwq = (w < 2) ? 3 : 2;
#pragma unroll
    for (int i = 0; i < 3; ++i) { const int b = (wq0 + i) * 1024 + lane * 16, k = b / 160, cb = b % 160; ws_[i] = (const char*)(wq + (size_t)k * D + hg * 80) + cb; }
    const int kv0 = (w == 2) ? 0 : 2, nkv = (w == 2) ? 2 : (w == 3 ? 1 : 0);
#pragma unroll
    for (int i = 0; i < 2; ++i) { int b = (kv0 + i) * 1024 + lane * 16; if (b >= 2560) b = 0; ks[i] = (const char*)wkv + b; }
    auto issue = [&](int t) {
        const unsigned sb = lds0 + (t % 3) * PSTG;
#pragma unroll
        for (int i = 0; i < 2; ++i) glds16(xs[i] + t * 128, rfl(sb + PX + (2 * w + i) * 1024));
        if constexpr (DOQ) {
#pragma unroll
            for (int i = 0; i < 3; ++i) if (i < nwq) glds16(ws_[i] + (size_t)t * 64 * D * 2, rfl(sb + PWQ + (wq0 + i) * 1024));
        }
        if constexpr (DOKV) {
#pragma unroll
            for (int i = 0; i < 2; ++i) if (i < nkv) glds16(ks[i] + t * 2560, rfl(sb + PWKV + (kv0 + i) * 1024));
        }
    };
    issue(0); issue(1);
    if constexpr (DOKV) {
        if (isV) for (int k = tid; k < D; k += 256) { u32x2 v = {(unsigned)gatew[k], 0u}; *(LAS u32x2*)(L + PGATE + k * 8) = v; }
    }
    const int cw = w & 1, hp = w >> 1, blk = (lane >> 4) & 1, q = (lane & 15) >> 2, p = lane & 3;
    const int xrow = cw * 32 + r, xrowk = kvhalf * 32 + r;
    const int xoff = PX + xrow * 128, xsw = (xrow >> 1) & 7, xoffk = PX + xrowk * 128, xswk = (xrowk >> 1) & 7;
    const int wqoff = PWQ + (8 * h + q) * 160 + (hp * 2 * 20 + 16 * blk + 4 * p) * 2;
    const bool gl = (blk == 1 && p >= 1);
    const int kvoff = PWKV + (8 * h + q) * 40 + (16 * blk + (gl ? 0 : 4 * p)) * 2;
    const int goff = PGATE + (8 * h + q) * 8;
    accq[0] = f32x16{}; accq[1] = f32x16{}; acckv = f32x16{};
    for (int t = 0; t < 10; ++t) {
        if constexpr (DOQ) { if (t < 9) WAITBAR(4); else WAITBAR(0); } else { if (t < 9) WAITBAR(2); else WAITBAR(0); }
        if (t + 2 < 10) issue(t + 2);
        lds_ptr sb = L + (t % 3) * PSTG;
#pragma unroll
        for (int s = 0; s < 4; ++s) {
            if constexpr (DOQ) {
                const bf16x8 xf = ldsv(sb + xoff + (((2 * s + h) ^ xsw) << 4));
#pragma unroll
                for (int a = 0; a < 2; ++a) { const bf16x8 wf = tr2(sb + wqoff + s * 16 * 160 + a * 40, 4 * 160); accq[a] = MFMA16(wf, xf, accq[a]); }
            }
            if constexpr (DOKV) {
                if (s == w) {
                    const bf16x8 xk = ldsv(sb + xoffk + (((2 * s + h) ^ xswk) << 4));
                    const bool useg = isV && blk == 1 && p == 1;
                    lds_ptr a0 = useg ? (L + goff + (t * 64 + s * 16) * 8) : (sb + kvoff + s * 16 * 40);
                    lds_ptr a1 = useg ? a0 + 4 * 8 : a0 + 4 * 40;
                    const s16x4 lo = trr(a0), hi = trr(a1);
                    const bf16x8 wf = __builtin_shufflevector(lo, hi, 0, 1, 2, 3, 4, 5, 6, 7);
                    if (isV) acckv = MFMA16(xk, wf, acckv); else acckv = MFMA16(wf, xk, acckv);
                }
            }
        }
    }
}

DI void kv_finish(f32x16 acc, char* smem, char* ws, int keytile, bool isV) {
    const int tid = threadIdx.x, lane = tid & 63, w = rfl(tid >> 6), r = lane & 31, h = lane >> 5;
    lds_ptr L = (lds_ptr)smem;
    asm volatile("s_waitcnt lgkmcnt(0)\n\ts_barrier" ::: "memory");
#pragma unroll
    for (int g = 0; g < 4; ++g) { f32x4 v = {acc[4 * g], acc[4 * g + 1], acc[4 * g + 2], acc[4 * g + 3]}; *(LAS f32x4*)(L + ((w * 4 + g) * 64 + lane) * 16) = v; }
    asm volatile("s_waitcnt lgkmcnt(0)\n\ts_barrier" ::: "memory");
    if (w == 0) {
        float a[16];
#pragma unroll
        for (int g = 0; g < 4; ++g) { f32x4 s = *(LAS f32x4*)(L + (g * 64 + lane) * 16);
#pragma unroll
            for (int ww = 1; ww < 4; ++ww) s += *(LAS f32x4*)(L + ((ww * 4 + g) * 64 + lane) * 16);
            a[4 * g] = s[0]; a[4 * g + 1] = s[1]; a[4 * g + 2] = s[2]; a[4 * g + 3] = s[3]; }
        char* t = ws + WS_KV + (size_t)keytile * KVT;
        if (!isV) {
            u32x4 k1 = {cvtpk(a[0], a[1]), cvtpk(a[2], a[3]), cvtpk(a[4], a[5]), cvtpk(a[6], a[7])};
            u32x2 k2 = h == 0 ? u32x2{cvtpk(a[8], a[9]), cvtpk(a[10], a[11])} : u32x2{0x3F803F80u, 0x00003F80u};
            *(u32x4*)(t + lane * 16) = k1; *(u32x2*)(t + 1024 + lane * 8) = k2;
        } else {
            u32x4 v0 = {cvtpk(a[0], a[1]), cvtpk(a[2], a[3]), cvtpk(a[4], a[5]), cvtpk(a[6], a[7])};
            u32x4 v1 = {cvtpk(a[8], a[9]), cvtpk(a[10], a[11]), cvtpk(a[12], a[13]), cvtpk(a[14], a[15])};
            if (r == 20) { v0 = u32x4{0x3F803F80u, 0x3F803F80u, 0x3F803F80u, 0x3F803F80u}; v1 = v0; } else if (r > 20) { v0 = u32x4{0, 0, 0, 0}; v1 = v0; }
            *(u32x4*)(t + 1536 + lane * 16) = v0; *(u32x4*)(t + 2560 + lane * 16) = v1;
            if (r == 20) { float* g = (float*)(ws + WS_G) + keytile * 32;
#pragma unroll
                for (int i = 0; i < 16; ++i) { const float lg = bf16r(a[i]); const float sg = bf16r(1.f / (1.f + __expf(-lg))); g[(i & 3) + 8 * (i >> 2) + 4 * h] = sg; } }
        }
    }
}

__global__ void __launch_bounds__(256, 2) k_kv(const uint16_t* x, const uint16_t* wk, const uint16_t* wv, const uint16_t* gatew, char* ws) {
    extern __shared__ __attribute__((aligned(16))) char smem[];
    const int tt = blockIdx.x >> 2, duty = blockIdx.x & 3, half = duty & 1; const bool isV = duty >> 1;
    f32x16 accq[2], acckv;
    proj_phase<false, true>(x, nullptr, isV ? wv : wk, gatew, tt, 0, half, isV, smem, accq, acckv);
    kv_finish(acckv, smem, ws, tt * 2 + half, isV);
}

struct QF { bf16x8 q1; s16x4 q2; };
DI float halfswap_max(float m) { auto rr = __builtin_amdgcn_permlane32_swap(__float_as_uint(m), __float_as_uint(m), false, false); return fmaxf(__uint_as_float(rr[0]), __uint_as_float(rr[1])); }
DI void set_shift(QF& q, float m, int h) {
    const float nm = -m, a = bf16r(nm), b = bf16r(nm - a), c = bf16r(nm - a - b);
    if (h) { u32x2 v = {cvtpk(a, b), cvtpk(c, 0.f)}; q.q2 = __builtin_bit_cast(s16x4, v); }
}
DI void attn_exact(QF (&q)[2], f32x16 (&o)[2], const char* kv, int lane) {
    const int h = lane >> 5;
#pragma unroll
    for (int c = 0; c < 2; ++c) if (h) q[c].q2 = s16x4{0, 0, 0, 0};
    float m[2] = {-INFINITY, -INFINITY};
    for (int t = 0; t < 64; ++t) {
        const char* p = kv + t * KVT; const bf16x8 k1 = *(const bf16x8*)(p + lane * 16); const s16x4 k2 = *(const s16x4*)(p + 1024 + lane * 8);
#pragma unroll
        for (int c = 0; c < 2; ++c) { f32x16 s = MFMA16(k1, q[c].q1, f32x16{}); s = MFMA8(k2, q[c].q2, s);
#pragma unroll
            for (int i = 0; i < 16; ++i) m[c] = fmaxf(m[c], s[i]); }
    }
#pragma unroll
    for (int c = 0; c < 2; ++c) { set_shift(q[c], halfswap_max(m[c]), h); o[c] = f32x16{}; }
    for (int t = 0; t < 64; ++t) {
        const char* p = kv + t * KVT; const bf16x8 k1 = *(const bf16x8*)(p + lane * 16); const s16x4 k2 = *(const s16x4*)(p + 1024 + lane * 8);
        const bf16x8 v0 = *(const bf16x8*)(p + 1536 + lane * 16), v1 = *(const bf16x8*)(p + 2560 + lane * 16);
#pragma unroll
        for (int c = 0; c < 2; ++c) { f32x16 s = MFMA16(k1, q[c].q1, f32x16{}); s = MFMA8(k2, q[c].q2, s);
            unsigned pk[8];
#pragma unroll
            for (int i = 0; i < 8; ++i) pk[i] = cvtpk(__builtin_amdgcn_exp2f(s[2 * i]), __builtin_amdgcn_exp2f(s[2 * i + 1]));
            const u32x4 p0 = {pk[0], pk[1], pk[2], pk[3]}, p1 = {pk[4], pk[5], pk[6], pk[7]};
            o[c] = MFMA16(v0, __builtin_bit_cast(bf16x8, p0), o[c]); o[c] = MFMA16(v1, __builtin_bit_cast(bf16x8, p1), o[c]); }
    }
}
DI void attn_store(f32x16 (&o)[2], char* ws, int tt, int hg, int cw, int hp, int lane) {
    const int r = lane & 31, h = lane >> 5, tile = tt * 2 + cw;
    const float g = ((const float*)(ws + WS_G))[tile * 32 + r];
    char* blk = ws + WS_O + ((size_t)tile * 40 + hg * 5) * 1024;
    unsigned d4[2][2];
#pragma unroll
    for (int c = 0; c < 2; ++c) {
        auto rr = __builtin_amdgcn_permlane32_swap(__float_as_uint(o[c][8]), __float_as_uint(o[c][8]), false, false);
        const float sc = g / __uint_as_float(rr[1]);
        u32x4 v = {cvtpk(o[c][0] * sc, o[c][1] * sc), cvtpk(o[c][2] * sc, o[c][3] * sc), cvtpk(o[c][4] * sc, o[c][5] * sc), cvtpk(o[c][6] * sc, o[c][7] * sc)};
        *(u32x4*)(blk + (hp * 2 + c) * 1024 + lane * 16) = v;
        d4[c][0] = cvtpk(o[c][8] * sc, o[c][9] * sc); d4[c][1] = cvtpk(o[c][10] * sc, o[c][11] * sc);
    }
    if (h == 0) { u32x4 v = {d4[0][0], d4[0][1], d4[1][0], d4[1][1]}; *(u32x4*)(blk + 4 * 1024 + (hp * 32 + r) * 16) = v; }
}

DI void role_attn(const uint16_t* x, const uint16_t* wq, char* ws, int b, char* smem) {
    const int tt = b >> 3, hg = b & 7, tid = threadIdx.x, lane = tid & 63, w = rfl(tid >> 6), h = lane >> 5, cw = w & 1, hp = w >> 1;
    f32x16 accq[2], acckv;
    proj_phase<true, false>(x, wq, nullptr, nullptr, tt, hg, 0, false, smem, accq, acckv);
    QF q[2];
#pragma unroll
    for (int c = 0; c < 2; ++c) {
        u32x4 a = {cvtpk(accq[c][0] * QSCALE, accq[c][1] * QSCALE), cvtpk(accq[c][2] * QSCALE, accq[c][3] * QSCALE), cvtpk(accq[c][4] * QSCALE, accq[c][5] * QSCALE), cvtpk(accq[c][6] * QSCALE, accq[c][7] * QSCALE)};
        u32x2 bq = {cvtpk(accq[c][8] * QSCALE, accq[c][9] * QSCALE), cvtpk(accq[c][10] * QSCALE, accq[c][11] * QSCALE)};
        q[c].q1 = __builtin_bit_cast(bf16x8, a); q[c].q2 = __builtin_bit_cast(s16x4, bq);
    }
    f32x16 o[2];
    attn_exact(q, o, ws + WS_KV, lane);
    attn_store(o, ws, tt, hg, cw, hp, lane);
}

constexpr int UX = 0, UW = 8192, USTG = 22528;
DI float geglu(float g, float u) {
    const float z = g * (-2.302208198f - 0.1029432397f * g * g);
    return g * __builtin_amdgcn_rcpf(1.f + __builtin_amdgcn_exp2f(z)) * u;
}
DI void role_ffn_up(const uint16_t* x, const uint16_t* wg, const uint16_t* wu, char* ws, int b, char* smem) {
    const int pp = b >> 4, cg = b & 15, tid = threadIdx.x, lane = tid & 63, w = rfl(tid >> 6), r = lane & 31, h = lane >> 5;
    const unsigned lds0 = (unsigned)(uintptr_t)smem; lds_ptr L = (lds_ptr)smem;
    const char* xs[2]; const char* wsrc[4];
#pragma unroll
    for (int i = 0; i < 2; ++i) { const int row = (2 * w + i) * 16 + (lane >> 2), c = (lane & 3) ^ ((row >> 2) & 3); xs[i] = (const char*)(x + (size_t)(pp * 128 + row) * D) + c * 16; }
    const int w0 = (w < 2) ? 4 * w : 8 + 3 * (w - 2), nw = (w < 2) ? 4 : 3;
    const int maxpc = (cg == 15) ? 5 : 13;
#pragma unroll
    for (int i = 0; i < 4; ++i) { const int bb = (w0 + i) * 1024 + lane * 16, k = bb / 448, rem = bb % 448, up = rem / 224; int pc = (rem % 224) / 16; if (pc > maxpc) pc = maxpc;
        wsrc[i] = (const char*)((up ? wu : wg) + (size_t)k * F + cg * 112) + pc * 16; }
    auto issue = [&](int t) {
        const unsigned sb = lds0 + (t % 3) * USTG;
#pragma unroll
        for (int i = 0; i < 2; ++i) glds16(xs[i] + t * 64, rfl(sb + UX + (2 * w + i) * 1024));
#pragma unroll
        for (int i = 0; i < 4; ++i) if (i < nw) glds16(wsrc[i] + (size_t)t * 32 * F * 2, rfl(sb + UW + (w0 + i) * 1024));
    };
    issue(0); issue(1);
    const int blk = (lane >> 4) & 1, q = (lane & 15) >> 2, p = lane & 3;
    const int xoff = UX + (w * 32 + r) * 64, xsw = (r >> 2) & 3;
    const int woff = UW + (8 * h + q) * 448 + (p >> 1) * 224 + (8 * blk + 4 * (p & 1)) * 2;
    f32x16 acc[7];
#pragma unroll
    for (int j = 0; j < 7; ++j) acc[j] = f32x16{};
    for (int t = 0; t < 20; ++t) {
        if (t < 19) WAITBAR(5); else WAITBAR(0);
        if (t + 2 < 20) issue(t + 2);
        lds_ptr sb = L + (t % 3) * USTG;
#pragma unroll
        for (int s = 0; s < 2; ++s) {
            const bf16x8 xf = ldsv(sb + xoff + (((2 * s + h) ^ xsw) << 4));
#pragma unroll
            for (int j = 0; j < 7; ++j) { const bf16x8 wf = tr2(sb + woff + s * 16 * 448 + j * 32, 4 * 448); acc[j] = MFMA16(wf, xf, acc[j]); }
        }
    }
    char* hb = ws + WS_H + ((size_t)(pp * 4 + w) * 108 + cg * 7) * 1024 + lane * 16;
    const int nj = (cg == 15) ? 3 : 7;
#pragma unroll
    for (int j = 0; j < 7; ++j) if (j < nj) {
        float hv[8];
#pragma unroll
        for (int e = 0; e < 4; ++e) { hv[e] = geglu(acc[j][e], acc[j][4 + e]); hv[4 + e] = geglu(acc[j][8 + e], acc[j][12 + e]); }
        u32x4 v = {cvtpk(hv[0], hv[1]), cvtpk(hv[2], hv[3]), cvtpk(hv[4], hv[5]), cvtpk(hv[6], hv[7])};
        *(u32x4*)(hb + j * 1024) = v;
    }
}

__global__ void __launch_bounds__(256, 2) k_main(const uint16_t* x, const uint16_t* wq, const uint16_t* wg, const uint16_t* wu, char* ws) {
    extern __shared__ __attribute__((aligned(16))) char smem[];
    if (blockIdx.x < 256) role_attn(x, wq, ws, blockIdx.x, smem);
    else role_ffn_up(x, wg, wu, ws, blockIdx.x - 256, smem);
}

constexpr int FSTG = 5120, FNS = 3;
__global__ void __launch_bounds__(256, 2) k_final(const uint16_t* wo, const uint16_t* wd, const char* ws, uint16_t* out) {
    extern __shared__ __attribute__((aligned(16))) char smem[];
    const int b = blockIdx.x, tt = b >> 3, ng = b & 7, tid = threadIdx.x, lane = tid & 63, w = rfl(tid >> 6), r = lane & 31, h = lane >> 5;
    const unsigned lds0 = (unsigned)(uintptr_t)smem + w * (FSTG * FNS + 64); lds_ptr L = (lds_ptr)smem + w * (FSTG * FNS + 64);
    const int blk = (lane >> 4) & 1, q = (lane & 15) >> 2, p = lane & 3;
    const int nH = (w < 2) ? 14 : 13, nT = nH + 5;
    int prow[5], pcol[5];
#pragma unroll
    for (int i = 0; i < 5; ++i) { const int bb = i * 1024 + lane * 16; prow[i] = bb / 160; pcol[i] = bb % 160; }
    auto orow = [&](int po, int rr) { const int sigma = 2 * po + (rr >> 4), hgp = sigma / 5, st = sigma % 5, i16 = rr & 15, second = i16 >> 3, hh = (i16 >> 2) & 1, e = i16 & 3;
        if (st < 4) return hgp * 80 + st * 20 + 8 * second + 4 * hh + e;
        return hgp * 80 + (2 * hh + second) * 20 + 16 + e; };
    auto issue = [&](int i) {
        const unsigned sb = lds0 + (i % FNS) * FSTG;
        if (i < nH) { const int pi = w + 4 * i;
#pragma unroll
            for (int j = 0; j < 5; ++j) glds16((const char*)(wd + (size_t)(pi * 32 + prow[j]) * D + ng * 80) + pcol[j], rfl(sb + j * 1024));
        } else { const int po = 5 * w + (i - nH);
#pragma unroll
            for (int j = 0; j < 5; ++j) glds16((const char*)(wo + (size_t)orow(po, prow[j]) * D + ng * 80) + pcol[j], rfl(sb + j * 1024));
        }
    };
    auto bsrc = [&](int i, int sub, int t2) -> const char* {
        if (i < nH) return ws + WS_H + ((size_t)(tt * 2 + t2) * 108 + (w + 4 * i) * 2 + sub) * 1024 + lane * 16;
        return ws + WS_O + ((size_t)(tt * 2 + t2) * 40 + (5 * w + (i - nH)) * 2 + sub) * 1024 + lane * 16; };
    f32x16 acc[2][3];
#pragma unroll
    for (int a = 0; a < 2; ++a)
#pragma unroll
        for (int c = 0; c < 3; ++c) acc[a][c] = f32x16{};
    const float* gp = (const float*)(ws + WS_G) + tt * 64;
    const float omg0 = bf16r(1.f - gp[r]), omg1 = bf16r(1.f - gp[32 + r]);
    const int aoff = (4 * h + q) * 160 + (16 * blk + 4 * p) * 2;
    for (int i = 0; i < nT; ++i) {
        issue(i);
        bf16x8 bf[2][2];
#pragma unroll
        for (int sub = 0; sub < 2; ++sub)
#pragma unroll
            for (int t2 = 0; t2 < 2; ++t2) bf[sub][t2] = *(const bf16x8*)bsrc(i, sub, t2);
        asm volatile("s_waitcnt vmcnt(0)" ::: "memory");
        if (i == nH) {
#pragma unroll
            for (int c = 0; c < 3; ++c) { acc[0][c] *= omg0; acc[1][c] *= omg1; }
        }
        lds_ptr sb = L + (i % FNS) * FSTG;
#pragma unroll
        for (int sub = 0; sub < 2; ++sub)
#pragma unroll
            for (int c = 0; c < 3; ++c) { const bf16x8 wf = tr2(sb + aoff + sub * 16 * 160 + c * 64, 8 * 160);
#pragma unroll
                for (int t2 = 0; t2 < 2; ++t2) acc[t2][c] = MFMA16(wf, bf[sub][t2], acc[t2][c]); }
        asm volatile("s_waitcnt lgkmcnt(0)" ::: "memory");
    }
    __syncthreads();
    auto grp = [&](int t2, int gq) -> f32x4 { const int c = gq >> 2, g4 = gq & 3; return f32x4{acc[t2][c][4 * g4], acc[t2][c][4 * g4 + 1], acc[t2][c][4 * g4 + 2], acc[t2][c][4 * g4 + 3]}; };
    lds_ptr R = (lds_ptr)smem;
#pragma unroll
    for (int t2 = 0; t2 < 2; ++t2)
#pragma unroll
        for (int gq = 0; gq < 10; ++gq) { const int owner = t2 | ((gq / 5) << 1);
            if (owner != w) { const int slot = (w - (w > owner ? 1 : 0)); *(LAS f32x4*)(R + ((owner * 3 + slot) * 5 + gq % 5) * 1024 + lane * 16) = grp(t2, gq); } }
    __syncthreads();
    const int t2o = w & 1, nh = w >> 1;
    f32x4 fin[5];
#pragma unroll
    for (int k = 0; k < 5; ++k) {
        f32x4 v = (t2o == 0) ? (nh == 0 ? grp(0, k) : grp(0, 5 + k)) : (nh == 0 ? grp(1, k) : grp(1, 5 + k));
#pragma unroll
        for (int s = 0; s < 3; ++s) v += *(LAS f32x4*)(R + ((w * 3 + s) * 5 + k) * 1024 + lane * 16);
        fin[k] = v;
    }
    uint16_t* orow_ = out + (size_t)(tt * 64 + t2o * 32 + r) * D + ng * 80 + nh * 40 + 4 * h;
#pragma unroll
    for (int k = 0; k < 5; ++k) { u32x2 v = {cvtpk(fin[k][0], fin[k][1]), cvtpk(fin[k][2], fin[k][3])}; *(u32x2*)(orow_ + 8 * k) = v; }
}

extern "C" void kernel_launch(void* const* d_in, const int* in_sizes, int n_in, void* d_out, int out_size, void* d_ws, size_t ws_size, hipStream_t stream) {
    const uint16_t* x = (const uint16_t*)d_in[0]; const uint16_t* wq = (const uint16_t*)d_in[1]; const uint16_t* wk = (const uint16_t*)d_in[2];
    const uint16_t* wv = (const uint16_t*)d_in[3]; const uint16_t* wo = (const uint16_t*)d_in[4]; const uint16_t* wg = (const uint16_t*)d_in[6];
    const uint16_t* wu = (const uint16_t*)d_in[7]; const uint16_t* wd = (const uint16_t*)d_in[8]; const uint16_t* gw = (const uint16_t*)d_in[9];
    char* ws = (char*)d_ws;
    (void)hipFuncSetAttribute((const void*)k_kv, hipFuncAttributeMaxDynamicSharedMemorySize, 81920);
    (void)hipFuncSetAttribute((const void*)k_main, hipFuncAttributeMaxDynamicSharedMemorySize, 81920);
    (void)hipFuncSetAttribute((const void*)k_final, hipFuncAttributeMaxDynamicSharedMemorySize, 81920);
    k_kv<<<128, 256, 81920, stream>>>(x, wk, wv, gw, ws);
    k_main<<<512, 256, 81920, stream>>>(x, wq, wg, wu, ws);
    k_final<<<256, 256, 81920, stream>>>(wo, wd, ws, (uint16_t*)d_out);
}
```

```cpp
#include <hip/hip_runtime.h>
#include <hip/hip_bf16.h>
#include <stdint.h>

typedef short bf16x8 __attribute__((ext_vector_type(8)));
typedef short s16x4 __attribute__((ext_vector_type(4)));
typedef short v4i16_t __attribute__((ext_vector_type(4)));
typedef float f32x16 __attribute__((ext_vector_type(16)));
typedef float f32x4 __attribute__((ext_vector_type(4)));
typedef float f32x2 __attribute__((ext_vector_type(2)));
typedef __bf16 bf16x2_t __attribute__((ext_vector_type(2)));
typedef unsigned u32x4 __attribute__((ext_vector_type(4)));
typedef unsigned u32x2 __attribute__((ext_vector_type(2)));
typedef unsigned long long u64;
#define LAS __attribute__((address_space(3)))
#define GAS __attribute__((address_space(1)))
#define DI __device__ __forceinline__
typedef LAS char* lds_ptr;
typedef __amdgpu_buffer_rsrc_t rsrc_t;

constexpr int S = 2048, D = 640, NH = 32, DH = 20, F = 1728;
constexpr int KVT = 3584;
constexpr unsigned WS_EPOCH = 0, WS_KVF = 1024, WS_HF = 2048, WS_RF = 8192, WS_AW = 24576;
constexpr unsigned WS_KV = 32768, WS_G = WS_KV + 65 * KVT, WS_H = 1u << 20, WS_O = WS_H + 64u * 108 * 1024, WS_WT = WS_O + 64u * 40 * 1024, WS_END = WS_WT + 8u * 74 * 5120;
constexpr float QSCALE = 0.22360679774997896f * 1.4426950408889634f;
constexpr int LDS_BYTES = 81920;

#define AP_Q 3
#define AP_ATT 1
#define GP_UP 2
#define GP_UP2 0
#define GP_DN 0
#define GP_HI 2
#define GP_RT 1
#define DNDELAY 200
#define PRIO(x) __builtin_amdgcn_s_setprio(x)
#define MFMA16(a, b, c) __builtin_amdgcn_mfma_f32_32x32x16_bf16((a), (b), (c), 0, 0, 0)
#define MFMA8(a, b, c) __builtin_amdgcn_mfma_f32_32x32x8bf16_1k((a), (b), (c), 0, 0, 0)
#define WAITBAR(N) asm volatile("s_waitcnt vmcnt(" #N ") lgkmcnt(0)\n\ts_barrier" ::: "memory")
#define LBAR() asm volatile("s_waitcnt lgkmcnt(0)\n\ts_barrier" ::: "memory")

DI unsigned cvtpk(float lo, float hi) { f32x2 v = {lo, hi}; bf16x2_t b = __builtin_convertvector(v, bf16x2_t); return __builtin_bit_cast(unsigned, b); }
DI float bf16r(float x) { return __uint_as_float(cvtpk(x, 0.f) << 16); }
DI s16x4 trr(lds_ptr p) { return __builtin_bit_cast(s16x4, __builtin_amdgcn_ds_read_tr16_b64_v4i16((LAS v4i16_t*)p)); }
DI bf16x8 tr2(lds_ptr p, int off2) { s16x4 lo = trr(p), hi = trr(p + off2); return __builtin_shufflevector(lo, hi, 0, 1, 2, 3, 4, 5, 6, 7); }
DI bf16x8 ldsv(lds_ptr p) { return *(LAS bf16x8*)p; }
DI unsigned rfl(unsigned x) { return __builtin_amdgcn_readfirstlane(x); }
DI int fresh_lane() { int l; asm volatile("v_mbcnt_lo_u32_b32 %0, -1, 0\n\tv_mbcnt_hi_u32_b32 %0, -1, %0" : "=v"(l)); return l; }
DI void glds_s(const void* sbase, unsigned voff, unsigned lds_dst) {
    asm volatile("s_mov_b32 m0, %2\n\ts_nop 0\n\tglobal_load_lds_dwordx4 %0, %1" :: "v"(voff), "s"(sbase), "s"(lds_dst) : "memory"); }
DI rsrc_t ws_rsrc(char* ws) { return __builtin_amdgcn_make_buffer_rsrc(ws, 0, WS_END, 0x00020000); }
DI void st16(rsrc_t r, unsigned off, u32x4 v) { __builtin_amdgcn_raw_buffer_store_b128(v, r, off, 0, 16); }
DI void st8(rsrc_t r, unsigned off, u32x2 v) { __builtin_amdgcn_raw_buffer_store_b64(v, r, off, 0, 16); }
DI void st4(rsrc_t r, unsigned off, unsigned v) { __builtin_amdgcn_raw_buffer_store_b32(v, r, off, 0, 16); }
DI u32x4 ld16(rsrc_t r, unsigned off) { return __builtin_amdgcn_raw_buffer_load_b128(r, off, 0, 16); }
DI u32x2 ld8(rsrc_t r, unsigned off) { return __builtin_amdgcn_raw_buffer_load_b64(r, off, 0, 16); }
DI unsigned ld4(rsrc_t r, unsigned off) { return __builtin_amdgcn_raw_buffer_load_b32(r, off, 0, 16); }
#define RLX_AGENT __ATOMIC_RELAXED, __HIP_MEMORY_SCOPE_AGENT
DI void publish(char* ws, unsigned off, unsigned tag, unsigned payload = 0u) {
    asm volatile("s_waitcnt vmcnt(0)\n\ts_barrier" ::: "memory");
    if (threadIdx.x == 0) __hip_atomic_store((GAS u64*)(ws + off), ((u64)tag << 32) | payload, RLX_AGENT);
}
template <int PER> DI unsigned poll(char* ws, const unsigned (&offs)[PER], unsigned tag) {
    unsigned pay = 0;
    for (unsigned spins = 0; spins < (1u << 18); ++spins) {
        u64 v[PER];
#pragma unroll
        for (int k = 0; k < PER; ++k) v[k] = __hip_atomic_load((GAS u64*)(ws + offs[k]), RLX_AGENT);
        bool ok = true; pay = 0;
#pragma unroll
        for (int k = 0; k < PER; ++k) { ok &= ((unsigned)(v[k] >> 32) == tag); pay = max(pay, (unsigned)v[k]); }
        if (__all(ok)) break;
        __builtin_amdgcn_s_sleep(1);
    }
    asm volatile("" ::: "memory");
    return pay;
}

DI unsigned wave_max_u32(unsigned v) {
    v = max(v, (unsigned)__builtin_amdgcn_update_dpp(0, (int)v, 0x111, 0xf, 0xf, true));
    v = max(v, (unsigned)__builtin_amdgcn_update_dpp(0, (int)v, 0x112, 0xf, 0xf, true));
    v = max(v, (unsigned)__builtin_amdgcn_update_dpp(0, (int)v, 0x114, 0xf, 0xf, true));
    v = max(v, (unsigned)__builtin_amdgcn_update_dpp(0, (int)v, 0x118, 0xf, 0xf, true));
    const unsigned a = __builtin_amdgcn_readlane(v, 15), b = __builtin_amdgcn_readlane(v, 31), c = __builtin_amdgcn_readlane(v, 47), d = __builtin_amdgcn_readlane(v, 63);
    return max(max(a, b), max(c, d));
}
DI unsigned make_tag(unsigned ep);
constexpr int DX = 0, DW = 40960;
typedef float f32x4v __attribute__((ext_vector_type(4)));
#define MFMAS(a, b, c) __builtin_amdgcn_mfma_f32_16x16x32_bf16((a), (b), (c), 0, 0, 0)
DI void kv_duty(const uint16_t* x, const uint16_t* wkv, const uint16_t* gatew, char* ws, int keytile, bool isV, unsigned ep, char* smem) {
    const int tid = threadIdx.x, lane = tid & 63, w = rfl(tid >> 6), c16 = lane & 15, kq = lane >> 4, th = w & 1, dh = w >> 1;
    const unsigned lds0 = (unsigned)(uintptr_t)smem; lds_ptr L = (lds_ptr)smem;
    {
        const char* xb = (const char*)(x + (size_t)keytile * 32 * D);
        const int row = w * 8 + (lane >> 3); const unsigned vo = row * 1280 + (((lane & 7) ^ ((row >> 1) & 7)) << 4);
#pragma unroll
        for (int kb = 0; kb < 10; ++kb) glds_s(xb + kb * 128, vo, lds0 + DX + kb * 4096 + w * 1024);
#pragma unroll
        for (int i = 0; i < 7; ++i) { const int pc = w + 4 * i; if (pc < 25) glds_s((const char*)wkv + pc * 1024, lane * 16, lds0 + DW + pc * 1024); }
    }
    const bool gl = isV && dh == 1 && c16 == 4;
    bf16x8 gf[20];
    if (isV && dh == 1) {
#pragma unroll
        for (int u = 0; u < 20; ++u) gf[u] = *(const bf16x8*)(gatew + u * 32 + kq * 8);
    }
    const int q = c16 >> 2, p = lane & 3;
    const int woff = DW + (8 * kq + q) * 40 + (16 * dh + ((dh == 1 && p >= 1) ? 0 : 4 * p)) * 2;
    const int xrow = th * 16 + c16, xoff = DX + xrow * 128, xsw = (xrow >> 1) & 7;
    WAITBAR(0);
    f32x4v acc[2] = {};
#pragma unroll
    for (int hb = 0; hb < 2; ++hb) {
        bf16x8 xk[10], wf[10];
#pragma unroll
        for (int i = 0; i < 10; ++i) { const int u = hb * 10 + i;
            xk[i] = ldsv(L + xoff + (u >> 1) * 4096 + (((4 * (u & 1) + kq) ^ xsw) << 4));
            wf[i] = tr2(L + woff + u * 32 * 40, 4 * 40);
        }
        __builtin_amdgcn_sched_barrier(0);
        if (!isV) {
#pragma unroll
            for (int i = 0; i < 10; ++i) acc[i & 1] = MFMAS(wf[i], xk[i], acc[i & 1]);
        } else if (dh == 0) {
#pragma unroll
            for (int i = 0; i < 10; ++i) acc[i & 1] = MFMAS(xk[i], wf[i], acc[i & 1]);
        } else {
#pragma unroll
            for (int i = 0; i < 10; ++i) acc[i & 1] = MFMAS(xk[i], gl ? gf[hb * 10 + i] : wf[i], acc[i & 1]);
        }
        __builtin_amdgcn_sched_barrier(0);
    }
    const f32x4v a = acc[0] + acc[1];
    const rsrc_t R = ws_rsrc(ws);
    const unsigned t = WS_KV + keytile * KVT;
    const u32x2 pk = {cvtpk(a[0], a[1]), cvtpk(a[2], a[3])};
    if (!isV) {
        const int tok = th * 16 + c16;
        if (dh == 0) st8(R, t + (tok + 32 * (kq & 1)) * 16 + (kq >> 1) * 8, pk);
        else if (kq == 0) st8(R, t + 1024 + tok * 8, pk);
        else if (kq == 1) st8(R, t + 1024 + (32 + tok) * 8, u32x2{0x3F803F80u, 0x00003F80u});
    } else {
        const int d = 16 * dh + c16;
        const u32x2 v = d < 20 ? pk : d == 20 ? u32x2{0x3F803F80u, 0x3F803F80u} : u32x2{0u, 0u};
        st8(R, t + 1536 + th * 1024 + (d + 32 * (kq & 1)) * 16 + (kq >> 1) * 8, v);
        if (gl) {
#pragma unroll
            for (int e = 0; e < 4; ++e) { const float lg = bf16r(a[e]); st4(R, WS_G + (keytile * 32 + th * 16 + 4 * kq + e) * 4, __float_as_uint(bf16r(__builtin_amdgcn_rcpf(1.f + __builtin_amdgcn_exp2f(-1.4426950408889634f * lg))))); }
        }
    }
    unsigned pay = 0;
    if (!isV) {
        float ss = a[0] * a[0] + a[1] * a[1] + a[2] * a[2] + a[3] * a[3];
        if (dh == 1 && kq != 0) ss = 0.f;
        { auto r16 = __builtin_amdgcn_permlane16_swap(__float_as_uint(ss), __float_as_uint(ss), false, false); ss = __uint_as_float(r16[0]) + __uint_as_float(r16[1]);
          auto r32 = __builtin_amdgcn_permlane32_swap(__float_as_uint(ss), __float_as_uint(ss), false, false); ss = __uint_as_float(r32[0]) + __uint_as_float(r32[1]); }
        if (kq == 0) *(LAS float*)(L + 70000 / 16 * 16 + (dh * 32 + th * 16 + c16) * 4) = ss;
        LBAR();
        if (w == 0) pay = wave_max_u32(__float_as_uint((*(LAS float*)(L + 70000 / 16 * 16 + (lane & 31) * 4) + *(LAS float*)(L + 70000 / 16 * 16 + (32 + (lane & 31)) * 4)) * 1.02f));
    }
    publish(ws, WS_KVF + (keytile * 2 + (isV ? 1 : 0)) * 8, make_tag(ep), pay);
}

constexpr int PX = 0, PWQ = 8192, PSTG = 18432;
#define QP_ASM \
  "v_mov_b32 v161, 0\n\t" \
  "global_load_dword v160, v161, s[78:79] sc1\n\t" \
  "s_add_u32 m0, s64, 0\n\t" \
  "s_nop 0\n\t" \
  "global_load_lds_dwordx4 v144, s[60:61]\n\t" \
  "s_add_u32 m0, s65, 0\n\t" \
  "s_nop 0\n\t" \
  "global_load_lds_dwordx4 v145, s[60:61]\n\t" \
  "s_add_u32 m0, s66, 0\n\t" \
  "s_nop 0\n\t" \
  "global_load_lds_dwordx4 v146, s[62:63]\n\t" \
  "s_add_u32 m0, s67, 0\n\t" \
  "s_nop 0\n\t" \
  "global_load_lds_dwordx4 v147, s[62:63]\n\t" \
  "s_add_u32 m0, s68, 0\n\t" \
  "s_nop 0\n\t" \
  "global_load_lds_dwordx4 v148, s[62:63]\n\t" \
  "s_add_u32 s60, s60, 128\n\t" \
  "s_addc_u32 s61, s61, 0\n\t" \
  "s_add_u32 s62, s62, 81920\n\t" \
  "s_addc_u32 s63, s63, 0\n\t" \
  "s_add_u32 m0, s64, 18432\n\t" \
  "s_nop 0\n\t" \
  "global_load_lds_dwordx4 v144, s[60:61]\n\t" \
  "s_add_u32 m0, s65, 18432\n\t" \
  "s_nop 0\n\t" \
  "global_load_lds_dwordx4 v145, s[60:61]\n\t" \
  "s_add_u32 m0, s66, 18432\n\t" \
  "s_nop 0\n\t" \
  "global_load_lds_dwordx4 v146, s[62:63]\n\t" \
  "s_add_u32 m0, s67, 18432\n\t" \
  "s_nop 0\n\t" \
  "global_load_lds_dwordx4 v147, s[62:63]\n\t" \
  "s_add_u32 m0, s68, 18432\n\t" \
  "s_nop 0\n\t" \
  "global_load_lds_dwordx4 v148, s[62:63]\n\t" \
  "s_add_u32 s60, s60, 128\n\t" \
  "s_addc_u32 s61, s61, 0\n\t" \
  "s_add_u32 s62, s62, 81920\n\t" \
  "s_addc_u32 s63, s63, 0\n\t" \
  "s_add_u32 m0, s64, 36864\n\t" \
  "s_nop 0\n\t" \
  "global_load_lds_dwordx4 v144, s[60:61]\n\t" \
  "s_add_u32 m0, s65, 36864\n\t" \
  "s_nop 0\n\t" \
  "global_load_lds_dwordx4 v145, s[60:61]\n\t" \
  "s_add_u32 m0, s66, 36864\n\t" \
  "s_nop 0\n\t" \
  "global_load_lds_dwordx4 v146, s[62:63]\n\t" \
  "s_add_u32 m0, s67, 36864\n\t" \
  "s_nop 0\n\t" \
  "global_load_lds_dwordx4 v147, s[62:63]\n\t" \
  "s_add_u32 m0, s68, 36864\n\t" \
  "s_nop 0\n\t" \
  "global_load_lds_dwordx4 v148, s[62:63]\n\t" \
  "s_add_u32 s60, s60, 128\n\t" \
  "s_addc_u32 s61, s61, 0\n\t" \
  "s_add_u32 s62, s62, 81920\n\t" \
  "s_addc_u32 s63, s63, 0\n\t" \
  "s_add_u32 m0, s64, 55296\n\t" \
  "s_nop 0\n\t" \
  "global_load_lds_dwordx4 v144, s[60:61]\n\t" \
  "s_add_u32 m0, s65, 55296\n\t" \
  "s_nop 0\n\t" \
  "global_load_lds_dwordx4 v145, s[60:61]\n\t" \
  "s_add_u32 m0, s66, 55296\n\t" \
  "s_nop 0\n\t" \
  "global_load_lds_dwordx4 v146, s[62:63]\n\t" \
  "s_add_u32 m0, s67, 55296\n\t" \
  "s_nop 0\n\t" \
  "global_load_lds_dwordx4 v147, s[62:63]\n\t" \
  "s_add_u32 m0, s68, 55296\n\t" \
  "s_nop 0\n\t" \
  "global_load_lds_dwordx4 v148, s[62:63]\n\t" \
  "s_add_u32 s60, s60, 128\n\t" \
  "s_addc_u32 s61, s61, 0\n\t" \
  "s_add_u32 s62, s62, 81920\n\t" \
  "s_addc_u32 s63, s63, 0\n\t" \
  "s_waitcnt vmcnt(15)\n\t" \
  "s_barrier\n\t" \
  "v_readfirstlane_b32 s80, v160\n\t" \
  "ds_read_b128 v[48:51], v150 offset:0\n\t" \
  "ds_read_b64_tr_b16 v[64:65], v154 offset:0\n\t" \
  "ds_read_b64_tr_b16 v[66:67], v154 offset:640\n\t" \
  "ds_read_b64_tr_b16 v[68:69], v154 offset:40\n\t" \
  "ds_read_b64_tr_b16 v[70:71], v154 offset:680\n\t" \
  "ds_read_b128 v[52:55], v151 offset:0\n\t" \
  "ds_read_b64_tr_b16 v[72:73], v154 offset:2560\n\t" \
  "ds_read_b64_tr_b16 v[74:75], v154 offset:3200\n\t" \
  "ds_read_b64_tr_b16 v[76:77], v154 offset:2600\n\t" \
  "ds_read_b64_tr_b16 v[78:79], v154 offset:3240\n\t" \
  "ds_read_b128 v[56:59], v152 offset:0\n\t" \
  "ds_read_b64_tr_b16 v[80:81], v154 offset:5120\n\t" \
  "ds_read_b64_tr_b16 v[82:83], v154 offset:5760\n\t" \
  "ds_read_b64_tr_b16 v[84:85], v154 offset:5160\n\t" \
  "ds_read_b64_tr_b16 v[86:87], v154 offset:5800\n\t" \
  "ds_read_b128 v[60:63], v153 offset:0\n\t" \
  "ds_read_b64_tr_b16 v[88:89], v154 offset:7680\n\t" \
  "ds_read_b64_tr_b16 v[90:91], v154 offset:8320\n\t" \
  "ds_read_b64_tr_b16 v[92:93], v154 offset:7720\n\t" \
  "ds_read_b64_tr_b16 v[94:95], v154 offset:8360\n\t" \
  "s_waitcnt vmcnt(10) lgkmcnt(0)\n\t" \
  "s_barrier\n\t" \
  "v_mfma_f32_32x32x16_bf16 v[16:31], v[64:67], v[48:51], v[16:31]\n\t" \
  "ds_read_b128 v[96:99], v150 offset:18432\n\t" \
  "ds_read_b64_tr_b16 v[112:113], v154 offset:18432\n\t" \
  "ds_read_b64_tr_b16 v[114:115], v154 offset:19072\n\t" \
  "ds_read_b64_tr_b16 v[116:117], v154 offset:18472\n\t" \
  "ds_read_b64_tr_b16 v[118:119], v154 offset:19112\n\t" \
  "v_mfma_f32_32x32x16_bf16 v[32:47], v[68:71], v[48:51], v[32:47]\n\t" \
  "ds_read_b128 v[100:103], v151 offset:18432\n\t" \
  "ds_read_b64_tr_b16 v[120:121], v154 offset:20992\n\t" \
  "ds_read_b64_tr_b16 v[122:123], v154 offset:21632\n\t" \
  "ds_read_b64_tr_b16 v[124:125], v154 offset:21032\n\t" \
  "v_mfma_f32_32x32x16_bf16 v[16:31], v[72:75], v[52:55], v[16:31]\n\t" \
  "ds_read_b64_tr_b16 v[126:127], v154 offset:21672\n\t" \
  "ds_read_b128 v[104:107], v152 offset:18432\n\t" \
  "ds_read_b64_tr_b16 v[128:129], v154 offset:23552\n\t" \
  "ds_read_b64_tr_b16 v[130:131], v154 offset:24192\n\t" \
  "v_mfma_f32_32x32x16_bf16 v[32:47], v[76:79], v[52:55], v[32:47]\n\t" \
  "ds_read_b64_tr_b16 v[132:133], v154 offset:23592\n\t" \
  "ds_read_b64_tr_b16 v[134:135], v154 offset:24232\n\t" \
  "ds_read_b128 v[108:111], v153 offset:18432\n\t" \
  "ds_read_b64_tr_b16 v[136:137], v154 offset:26112\n\t" \
  "ds_read_b64_tr_b16 v[138:139], v154 offset:26752\n\t" \
  "v_mfma_f32_32x32x16_bf16 v[16:31], v[80:83], v[56:59], v[16:31]\n\t" \
  "ds_read_b64_tr_b16 v[140:141], v154 offset:26152\n\t" \
  "ds_read_b64_tr_b16 v[142:143], v154 offset:26792\n\t" \
  "s_add_u32 m0, s64, 0\n\t" \
  "s_nop 0\n\t" \
  "global_load_lds_dwordx4 v144, s[60:61]\n\t" \
  "s_add_u32 m0, s65, 0\n\t" \
  "s_nop 0\n\t" \
  "global_load_lds_dwordx4 v145, s[60:61]\n\t" \
  "v_mfma_f32_32x32x16_bf16 v[32:47], v[84:87], v[56:59], v[32:47]\n\t" \
  "s_add_u32 m0, s66, 0\n\t" \
  "s_nop 0\n\t" \
  "global_load_lds_dwordx4 v146, s[62:63]\n\t" \
  "s_add_u32 m0, s67, 0\n\t" \
  "s_nop 0\n\t" \
  "global_load_lds_dwordx4 v147, s[62:63]\n\t" \
  "s_add_u32 m0, s68, 0\n\t" \
  "s_nop 0\n\t" \
  "global_load_lds_dwordx4 v148, s[62:63]\n\t" \
  "s_add_u32 s60, s60, 128\n\t" \
  "s_addc_u32 s61, s61, 0\n\t" \
  "s_add_u32 s62, s62, 81920\n\t" \
  "s_addc_u32 s63, s63, 0\n\t" \
  "v_mfma_f32_32x32x16_bf16 v[16:31], v[88:91], v[60:63], v[16:31]\n\t" \
  "v_mfma_f32_32x32x16_bf16 v[32:47], v[92:95], v[60:63], v[32:47]\n\t" \
  "s_waitcnt vmcnt(10) lgkmcnt(0)\n\t" \
  "s_barrier\n\t" \
  "v_mfma_f32_32x32x16_bf16 v[16:31], v[112:115], v[96:99], v[16:31]\n\t" \
  "ds_read_b128 v[48:51], v150 offset:36864\n\t" \
  "ds_read_b64_tr_b16 v[64:65], v154 offset:36864\n\t" \
  "ds_read_b64_tr_b16 v[66:67], v154 offset:37504\n\t" \
  "ds_read_b64_tr_b16 v[68:69], v154 offset:36904\n\t" \
  "ds_read_b64_tr_b16 v[70:71], v154 offset:37544\n\t" \
  "v_mfma_f32_32x32x16_bf16 v[32:47], v[116:119], v[96:99], v[32:47]\n\t" \
  "ds_read_b128 v[52:55], v151 offset:36864\n\t" \
  "ds_read_b64_tr_b16 v[72:73], v154 offset:39424\n\t" \
  "ds_read_b64_tr_b16 v[74:75], v154 offset:40064\n\t" \
  "ds_read_b64_tr_b16 v[76:77], v154 offset:39464\n\t" \
  "v_mfma_f32_32x32x16_bf16 v[16:31], v[120:123], v[100:103], v[16:31]\n\t" \
  "ds_read_b64_tr_b16 v[78:79], v154 offset:40104\n\t" \
  "ds_read_b128 v[56:59], v152 offset:36864\n\t" \
  "ds_read_b64_tr_b16 v[80:81], v154 offset:41984\n\t" \
  "ds_read_b64_tr_b16 v[82:83], v154 offset:42624\n\t" \
  "v_mfma_f32_32x32x16_bf16 v[32:47], v[124:127], v[100:103], v[32:47]\n\t" \
  "ds_read_b64_tr_b16 v[84:85], v154 offset:42024\n\t" \
  "ds_read_b64_tr_b16 v[86:87], v154 offset:42664\n\t" \
  "ds_read_b128 v[60:63], v153 offset:36864\n\t" \
  "ds_read_b64_tr_b16 v[88:89], v154 offset:44544\n\t" \
  "ds_read_b64_tr_b16 v[90:91], v154 offset:45184\n\t" \
  "v_mfma_f32_32x32x16_bf16 v[16:31], v[128:131], v[104:107], v[16:31]\n\t" \
  "ds_read_b64_tr_b16 v[92:93], v154 offset:44584\n\t" \
  "ds_read_b64_tr_b16 v[94:95], v154 offset:45224\n\t" \
  "s_add_u32 m0, s64, 18432\n\t" \
  "s_nop 0\n\t" \
  "global_load_lds_dwordx4 v144, s[60:61]\n\t" \
  "s_add_u32 m0, s65, 18432\n\t" \
  "s_nop 0\n\t" \
  "global_load_lds_dwordx4 v145, s[60:61]\n\t" \
  "v_mfma_f32_32x32x16_bf16 v[32:47], v[132:135], v[104:107], v[32:47]\n\t" \
  "s_add_u32 m0, s66, 18432\n\t" \
  "s_nop 0\n\t" \
  "global_load_lds_dwordx4 v146, s[62:63]\n\t" \
  "s_add_u32 m0, s67, 18432\n\t" \
  "s_nop 0\n\t" \
  "global_load_lds_dwordx4 v147, s[62:63]\n\t" \
  "s_add_u32 m0, s68, 18432\n\t" \
  "s_nop 0\n\t" \
  "global_load_lds_dwordx4 v148, s[62:63]\n\t" \
  "s_add_u32 s60, s60, 128\n\t" \
  "s_addc_u32 s61, s61, 0\n\t" \
  "s_add_u32 s62, s62, 81920\n\t" \
  "s_addc_u32 s63, s63, 0\n\t" \
  "v_mfma_f32_32x32x16_bf16 v[16:31], v[136:139], v[108:111], v[16:31]\n\t" \
  "v_mfma_f32_32x32x16_bf16 v[32:47], v[140:143], v[108:111], v[32:47]\n\t" \
  "s_waitcnt vmcnt(10) lgkmcnt(0)\n\t" \
  "s_barrier\n\t" \
  "v_mfma_f32_32x32x16_bf16 v[16:31], v[64:67], v[48:51], v[16:31]\n\t" \
  "ds_read_b128 v[96:99], v150 offset:55296\n\t" \
  "ds_read_b64_tr_b16 v[112:113], v154 offset:55296\n\t" \
  "ds_read_b64_tr_b16 v[114:115], v154 offset:55936\n\t" \
  "ds_read_b64_tr_b16 v[116:117], v154 offset:55336\n\t" \
  "ds_read_b64_tr_b16 v[118:119], v154 offset:55976\n\t" \
  "v_mfma_f32_32x32x16_bf16 v[32:47], v[68:71], v[48:51], v[32:47]\n\t" \
  "ds_read_b128 v[100:103], v151 offset:55296\n\t" \
  "ds_read_b64_tr_b16 v[120:121], v154 offset:57856\n\t" \
  "ds_read_b64_tr_b16 v[122:123], v154 offset:58496\n\t" \
  "ds_read_b64_tr_b16 v[124:125], v154 offset:57896\n\t" \
  "v_mfma_f32_32x32x16_bf16 v[16:31], v[72:75], v[52:55], v[16:31]\n\t" \
  "ds_read_b64_tr_b16 v[126:127], v154 offset:58536\n\t" \
  "ds_read_b128 v[104:107], v152 offset:55296\n\t" \
  "ds_read_b64_tr_b16 v[128:129], v154 offset:60416\n\t" \
  "ds_read_b64_tr_b16 v[130:131], v154 offset:61056\n\t" \
  "v_mfma_f32_32x32x16_bf16 v[32:47], v[76:79], v[52:55], v[32:47]\n\t" \
  "ds_read_b64_tr_b16 v[132:133], v154 offset:60456\n\t" \
  "ds_read_b64_tr_b16 v[134:135], v154 offset:61096\n\t" \
  "ds_read_b128 v[108:111], v153 offset:55296\n\t" \
  "ds_read_b64_tr_b16 v[136:137], v154 offset:62976\n\t" \
  "ds_read_b64_tr_b16 v[138:139], v154 offset:63616\n\t" \
  "v_mfma_f32_32x32x16_bf16 v[16:31], v[80:83], v[56:59], v[16:31]\n\t" \
  "ds_read_b64_tr_b16 v[140:141], v154 offset:63016\n\t" \
  "ds_read_b64_tr_b16 v[142:143], v154 offset:63656\n\t" \
  "s_add_u32 m0, s64, 36864\n\t" \
  "s_nop 0\n\t" \
  "global_load_lds_dwordx4 v144, s[60:61]\n\t" \
  "s_add_u32 m0, s65, 36864\n\t" \
  "s_nop 0\n\t" \
  "global_load_lds_dwordx4 v145, s[60:61]\n\t" \
  "v_mfma_f32_32x32x16_bf16 v[32:47], v[84:87], v[56:59], v[32:47]\n\t" \
  "s_add_u32 m0, s66, 36864\n\t" \
  "s_nop 0\n\t" \
  "global_load_lds_dwordx4 v146, s[62:63]\n\t" \
  "s_add_u32 m0, s67, 36864\n\t" \
  "s_nop 0\n\t" \
  "global_load_lds_dwordx4 v147, s[62:63]\n\t" \
  "s_add_u32 m0, s68, 36864\n\t" \
  "s_nop 0\n\t" \
  "global_load_lds_dwordx4 v148, s[62:63]\n\t" \
  "s_add_u32 s60, s60, 128\n\t" \
  "s_addc_u32 s61, s61, 0\n\t" \
  "s_add_u32 s62, s62, 81920\n\t" \
  "s_addc_u32 s63, s63, 0\n\t" \
  "v_mfma_f32_32x32x16_bf16 v[16:31], v[88:91], v[60:63], v[16:31]\n\t" \
  "v_mfma_f32_32x32x16_bf16 v[32:47], v[92:95], v[60:63], v[32:47]\n\t" \
  "s_waitcnt vmcnt(10) lgkmcnt(0)\n\t" \
  "s_barrier\n\t" \
  "v_mfma_f32_32x32x16_bf16 v[16:31], v[112:115], v[96:99], v[16:31]\n\t" \
  "ds_read_b128 v[48:51], v150 offset:0\n\t" \
  "ds_read_b64_tr_b16 v[64:65], v154 offset:0\n\t" \
  "ds_read_b64_tr_b16 v[66:67], v154 offset:640\n\t" \
  "ds_read_b64_tr_b16 v[68:69], v154 offset:40\n\t" \
  "ds_read_b64_tr_b16 v[70:71], v154 offset:680\n\t" \
  "v_mfma_f32_32x32x16_bf16 v[32:47], v[116:119], v[96:99], v[32:47]\n\t" \
  "ds_read_b128 v[52:55], v151 offset:0\n\t" \
  "ds_read_b64_tr_b16 v[72:73], v154 offset:2560\n\t" \
  "ds_read_b64_tr_b16 v[74:75], v154 offset:3200\n\t" \
  "ds_read_b64_tr_b16 v[76:77], v154 offset:2600\n\t" \
  "v_mfma_f32_32x32x16_bf16 v[16:31], v[120:123], v[100:103], v[16:31]\n\t" \
  "ds_read_b64_tr_b16 v[78:79], v154 offset:3240\n\t" \
  "ds_read_b128 v[56:59], v152 offset:0\n\t" \
  "ds_read_b64_tr_b16 v[80:81], v154 offset:5120\n\t" \
  "ds_read_b64_tr_b16 v[82:83], v154 offset:5760\n\t" \
  "v_mfma_f32_32x32x16_bf16 v[32:47], v[124:127], v[100:103], v[32:47]\n\t" \
  "ds_read_b64_tr_b16 v[84:85], v154 offset:5160\n\t" \
  "ds_read_b64_tr_b16 v[86:87], v154 offset:5800\n\t" \
  "ds_read_b128 v[60:63], v153 offset:0\n\t" \
  "ds_read_b64_tr_b16 v[88:89], v154 offset:7680\n\t" \
  "ds_read_b64_tr_b16 v[90:91], v154 offset:8320\n\t" \
  "v_mfma_f32_32x32x16_bf16 v[16:31], v[128:131], v[104:107], v[16:31]\n\t" \
  "ds_read_b64_tr_b16 v[92:93], v154 offset:7720\n\t" \
  "ds_read_b64_tr_b16 v[94:95], v154 offset:8360\n\t" \
  "s_add_u32 m0, s64, 55296\n\t" \
  "s_nop 0\n\t" \
  "global_load_lds_dwordx4 v144, s[60:61]\n\t" \
  "s_add_u32 m0, s65, 55296\n\t" \
  "s_nop 0\n\t" \
  "global_load_lds_dwordx4 v145, s[60:61]\n\t" \
  "v_mfma_f32_32x32x16_bf16 v[32:47], v[132:135], v[104:107], v[32:47]\n\t" \
  "s_add_u32 m0, s66, 55296\n\t" \
  "s_nop 0\n\t" \
  "global_load_lds_dwordx4 v146, s[62:63]\n\t" \
  "s_add_u32 m0, s67, 55296\n\t" \
  "s_nop 0\n\t" \
  "global_load_lds_dwordx4 v147, s[62:63]\n\t" \
  "s_add_u32 m0, s68, 55296\n\t" \
  "s_nop 0\n\t" \
  "global_load_lds_dwordx4 v148, s[62:63]\n\t" \
  "s_add_u32 s60, s60, 128\n\t" \
  "s_addc_u32 s61, s61, 0\n\t" \
  "s_add_u32 s62, s62, 81920\n\t" \
  "s_addc_u32 s63, s63, 0\n\t" \
  "v_mfma_f32_32x32x16_bf16 v[16:31], v[136:139], v[108:111], v[16:31]\n\t" \
  "v_mfma_f32_32x32x16_bf16 v[32:47], v[140:143], v[108:111], v[32:47]\n\t" \
  "s_waitcnt vmcnt(10) lgkmcnt(0)\n\t" \
  "s_barrier\n\t" \
  "v_mfma_f32_32x32x16_bf16 v[16:31], v[64:67], v[48:51], v[16:31]\n\t" \
  "ds_read_b128 v[96:99], v150 offset:18432\n\t" \
  "ds_read_b64_tr_b16 v[112:113], v154 offset:18432\n\t" \
  "ds_read_b64_tr_b16 v[114:115], v154 offset:19072\n\t" \
  "ds_read_b64_tr_b16 v[116:117], v154 offset:18472\n\t" \
  "ds_read_b64_tr_b16 v[118:119], v154 offset:19112\n\t" \
  "v_mfma_f32_32x32x16_bf16 v[32:47], v[68:71], v[48:51], v[32:47]\n\t" \
  "ds_read_b128 v[100:103], v151 offset:18432\n\t" \
  "ds_read_b64_tr_b16 v[120:121], v154 offset:20992\n\t" \
  "ds_read_b64_tr_b16 v[122:123], v154 offset:21632\n\t" \
  "ds_read_b64_tr_b16 v[124:125], v154 offset:21032\n\t" \
  "v_mfma_f32_32x32x16_bf16 v[16:31], v[72:75], v[52:55], v[16:31]\n\t" \
  "ds_read_b64_tr_b16 v[126:127], v154 offset:21672\n\t" \
  "ds_read_b128 v[104:107], v152 offset:18432\n\t" \
  "ds_read_b64_tr_b16 v[128:129], v154 offset:23552\n\t" \
  "ds_read_b64_tr_b16 v[130:131], v154 offset:24192\n\t" \
  "v_mfma_f32_32x32x16_bf16 v[32:47], v[76:79], v[52:55], v[32:47]\n\t" \
  "ds_read_b64_tr_b16 v[132:133], v154 offset:23592\n\t" \
  "ds_read_b64_tr_b16 v[134:135], v154 offset:24232\n\t" \
  "ds_read_b128 v[108:111], v153 offset:18432\n\t" \
  "ds_read_b64_tr_b16 v[136:137], v154 offset:26112\n\t" \
  "ds_read_b64_tr_b16 v[138:139], v154 offset:26752\n\t" \
  "v_mfma_f32_32x32x16_bf16 v[16:31], v[80:83], v[56:59], v[16:31]\n\t" \
  "ds_read_b64_tr_b16 v[140:141], v154 offset:26152\n\t" \
  "ds_read_b64_tr_b16 v[142:143], v154 offset:26792\n\t" \
  "s_add_u32 m0, s64, 0\n\t" \
  "s_nop 0\n\t" \
  "global_load_lds_dwordx4 v144, s[60:61]\n\t" \
  "s_add_u32 m0, s65, 0\n\t" \
  "s_nop 0\n\t" \
  "global_load_lds_dwordx4 v145, s[60:61]\n\t" \
  "v_mfma_f32_32x32x16_bf16 v[32:47], v[84:87], v[56:59], v[32:47]\n\t" \
  "s_add_u32 m0, s66, 0\n\t" \
  "s_nop 0\n\t" \
  "global_load_lds_dwordx4 v146, s[62:63]\n\t" \
  "s_add_u32 m0, s67, 0\n\t" \
  "s_nop 0\n\t" \
  "global_load_lds_dwordx4 v147, s[62:63]\n\t" \
  "s_add_u32 m0, s68, 0\n\t" \
  "s_nop 0\n\t" \
  "global_load_lds_dwordx4 v148, s[62:63]\n\t" \
  "s_add_u32 s60, s60, 128\n\t" \
  "s_addc_u32 s61, s61, 0\n\t" \
  "s_add_u32 s62, s62, 81920\n\t" \
  "s_addc_u32 s63, s63, 0\n\t" \
  "v_mfma_f32_32x32x16_bf16 v[16:31], v[88:91], v[60:63], v[16:31]\n\t" \
  "v_mfma_f32_32x32x16_bf16 v[32:47], v[92:95], v[60:63], v[32:47]\n\t" \
  "s_waitcnt vmcnt(10) lgkmcnt(0)\n\t" \
  "s_barrier\n\t" \
  "v_mfma_f32_32x32x16_bf16 v[16:31], v[112:115], v[96:99], v[16:31]\n\t" \
  "ds_read_b128 v[48:51], v150 offset:36864\n\t" \
  "ds_read_b64_tr_b16 v[64:65], v154 offset:36864\n\t" \
  "ds_read_b64_tr_b16 v[66:67], v154 offset:37504\n\t" \
  "ds_read_b64_tr_b16 v[68:69], v154 offset:36904\n\t" \
  "ds_read_b64_tr_b16 v[70:71], v154 offset:37544\n\t" \
  "v_mfma_f32_32x32x16_bf16 v[32:47], v[116:119], v[96:99], v[32:47]\n\t" \
  "ds_read_b128 v[52:55], v151 offset:36864\n\t" \
  "ds_read_b64_tr_b16 v[72:73], v154 offset:39424\n\t" \
  "ds_read_b64_tr_b16 v[74:75], v154 offset:40064\n\t" \
  "ds_read_b64_tr_b16 v[76:77], v154 offset:39464\n\t" \
  "v_mfma_f32_32x32x16_bf16 v[16:31], v[120:123], v[100:103], v[16:31]\n\t" \
  "ds_read_b64_tr_b16 v[78:79], v154 offset:40104\n\t" \
  "ds_read_b128 v[56:59], v152 offset:36864\n\t" \
  "ds_read_b64_tr_b16 v[80:81], v154 offset:41984\n\t" \
  "ds_read_b64_tr_b16 v[82:83], v154 offset:42624\n\t" \
  "v_mfma_f32_32x32x16_bf16 v[32:47], v[124:127], v[100:103], v[32:47]\n\t" \
  "ds_read_b64_tr_b16 v[84:85], v154 offset:42024\n\t" \
  "ds_read_b64_tr_b16 v[86:87], v154 offset:42664\n\t" \
  "ds_read_b128 v[60:63], v153 offset:36864\n\t" \
  "ds_read_b64_tr_b16 v[88:89], v154 offset:44544\n\t" \
  "ds_read_b64_tr_b16 v[90:91], v154 offset:45184\n\t" \
  "v_mfma_f32_32x32x16_bf16 v[16:31], v[128:131], v[104:107], v[16:31]\n\t" \
  "ds_read_b64_tr_b16 v[92:93], v154 offset:44584\n\t" \
  "ds_read_b64_tr_b16 v[94:95], v154 offset:45224\n\t" \
  "s_add_u32 m0, s64, 18432\n\t" \
  "s_nop 0\n\t" \
  "global_load_lds_dwordx4 v144, s[60:61]\n\t" \
  "s_add_u32 m0, s65, 18432\n\t" \
  "s_nop 0\n\t" \
  "global_load_lds_dwordx4 v145, s[60:61]\n\t" \
  "v_mfma_f32_32x32x16_bf16 v[32:47], v[132:135], v[104:107], v[32:47]\n\t" \
  "s_add_u32 m0, s66, 18432\n\t" \
  "s_nop 0\n\t" \
  "global_load_lds_dwordx4 v146, s[62:63]\n\t" \
  "s_add_u32 m0, s67, 18432\n\t" \
  "s_nop 0\n\t" \
  "global_load_lds_dwordx4 v147, s[62:63]\n\t" \
  "s_add_u32 m0, s68, 18432\n\t" \
  "s_nop 0\n\t" \
  "global_load_lds_dwordx4 v148, s[62:63]\n\t" \
  "s_add_u32 s60, s60, 128\n\t" \
  "s_addc_u32 s61, s61, 0\n\t" \
  "s_add_u32 s62, s62, 81920\n\t" \
  "s_addc_u32 s63, s63, 0\n\t" \
  "v_mfma_f32_32x32x16_bf16 v[16:31], v[136:139], v[108:111], v[16:31]\n\t" \
  "v_mfma_f32_32x32x16_bf16 v[32:47], v[140:143], v[108:111], v[32:47]\n\t" \
  "s_waitcnt vmcnt(10) lgkmcnt(0)\n\t" \
  "s_barrier\n\t" \
  "v_mfma_f32_32x32x16_bf16 v[16:31], v[64:67], v[48:51], v[16:31]\n\t" \
  "ds_read_b128 v[96:99], v150 offset:55296\n\t" \
  "ds_read_b64_tr_b16 v[112:113], v154 offset:55296\n\t" \
  "ds_read_b64_tr_b16 v[114:115], v154 offset:55936\n\t" \
  "ds_read_b64_tr_b16 v[116:117], v154 offset:55336\n\t" \
  "v_mfma_f32_32x32x16_bf16 v[32:47], v[68:71], v[48:51], v[32:47]\n\t" \
  "ds_read_b64_tr_b16 v[118:119], v154 offset:55976\n\t" \
  "ds_read_b128 v[100:103], v151 offset:55296\n\t" \
  "ds_read_b64_tr_b16 v[120:121], v154 offset:57856\n\t" \
  "v_mfma_f32_32x32x16_bf16 v[16:31], v[72:75], v[52:55], v[16:31]\n\t" \
  "ds_read_b64_tr_b16 v[122:123], v154 offset:58496\n\t" \
  "ds_read_b64_tr_b16 v[124:125], v154 offset:57896\n\t" \
  "ds_read_b64_tr_b16 v[126:127], v154 offset:58536\n\t" \
  "v_mfma_f32_32x32x16_bf16 v[32:47], v[76:79], v[52:55], v[32:47]\n\t" \
  "ds_read_b128 v[104:107], v152 offset:55296\n\t" \
  "ds_read_b64_tr_b16 v[128:129], v154 offset:60416\n\t" \
  "ds_read_b64_tr_b16 v[130:131], v154 offset:61056\n\t" \
  "ds_read_b64_tr_b16 v[132:133], v154 offset:60456\n\t" \
  "v_mfma_f32_32x32x16_bf16 v[16:31], v[80:83], v[56:59], v[16:31]\n\t" \
  "ds_read_b64_tr_b16 v[134:135], v154 offset:61096\n\t" \
  "ds_read_b128 v[108:111], v153 offset:55296\n\t" \
  "ds_read_b64_tr_b16 v[136:137], v154 offset:62976\n\t" \
  "v_mfma_f32_32x32x16_bf16 v[32:47], v[84:87], v[56:59], v[32:47]\n\t" \
  "ds_read_b64_tr_b16 v[138:139], v154 offset:63616\n\t" \
  "ds_read_b64_tr_b16 v[140:141], v154 offset:63016\n\t" \
  "ds_read_b64_tr_b16 v[142:143], v154 offset:63656\n\t" \
  "v_mfma_f32_32x32x16_bf16 v[16:31], v[88:91], v[60:63], v[16:31]\n\t" \
  "v_mfma_f32_32x32x16_bf16 v[32:47], v[92:95], v[60:63], v[32:47]\n\t" \
  "s_waitcnt vmcnt(5) lgkmcnt(0)\n\t" \
  "s_barrier\n\t" \
  "v_mfma_f32_32x32x16_bf16 v[16:31], v[112:115], v[96:99], v[16:31]\n\t" \
  "ds_read_b128 v[48:51], v150 offset:0\n\t" \
  "ds_read_b64_tr_b16 v[64:65], v154 offset:0\n\t" \
  "ds_read_b64_tr_b16 v[66:67], v154 offset:640\n\t" \
  "ds_read_b64_tr_b16 v[68:69], v154 offset:40\n\t" \
  "v_mfma_f32_32x32x16_bf16 v[32:47], v[116:119], v[96:99], v[32:47]\n\t" \
  "ds_read_b64_tr_b16 v[70:71], v154 offset:680\n\t" \
  "ds_read_b128 v[52:55], v151 offset:0\n\t" \
  "ds_read_b64_tr_b16 v[72:73], v154 offset:2560\n\t" \
  "v_mfma_f32_32x32x16_bf16 v[16:31], v[120:123], v[100:103], v[16:31]\n\t" \
  "ds_read_b64_tr_b16 v[74:75], v154 offset:3200\n\t" \
  "ds_read_b64_tr_b16 v[76:77], v154 offset:2600\n\t" \
  "ds_read_b64_tr_b16 v[78:79], v154 offset:3240\n\t" \
  "v_mfma_f32_32x32x16_bf16 v[32:47], v[124:127], v[100:103], v[32:47]\n\t" \
  "ds_read_b128 v[56:59], v152 offset:0\n\t" \
  "ds_read_b64_tr_b16 v[80:81], v154 offset:5120\n\t" \
  "ds_read_b64_tr_b16 v[82:83], v154 offset:5760\n\t" \
  "ds_read_b64_tr_b16 v[84:85], v154 offset:5160\n\t" \
  "v_mfma_f32_32x32x16_bf16 v[16:31], v[128:131], v[104:107], v[16:31]\n\t" \
  "ds_read_b64_tr_b16 v[86:87], v154 offset:5800\n\t" \
  "ds_read_b128 v[60:63], v153 offset:0\n\t" \
  "ds_read_b64_tr_b16 v[88:89], v154 offset:7680\n\t" \
  "v_mfma_f32_32x32x16_bf16 v[32:47], v[132:135], v[104:107], v[32:47]\n\t" \
  "ds_read_b64_tr_b16 v[90:91], v154 offset:8320\n\t" \
  "ds_read_b64_tr_b16 v[92:93], v154 offset:7720\n\t" \
  "ds_read_b64_tr_b16 v[94:95], v154 offset:8360\n\t" \
  "v_mfma_f32_32x32x16_bf16 v[16:31], v[136:139], v[108:111], v[16:31]\n\t" \
  "v_mfma_f32_32x32x16_bf16 v[32:47], v[140:143], v[108:111], v[32:47]\n\t" \
  "s_waitcnt vmcnt(0) lgkmcnt(0)\n\t" \
  "s_barrier\n\t" \
  "v_mfma_f32_32x32x16_bf16 v[16:31], v[64:67], v[48:51], v[16:31]\n\t" \
  "ds_read_b128 v[96:99], v150 offset:18432\n\t" \
  "ds_read_b64_tr_b16 v[112:113], v154 offset:18432\n\t" \
  "ds_read_b64_tr_b16 v[114:115], v154 offset:19072\n\t" \
  "ds_read_b64_tr_b16 v[116:117], v154 offset:18472\n\t" \
  "v_mfma_f32_32x32x16_bf16 v[32:47], v[68:71], v[48:51], v[32:47]\n\t" \
  "ds_read_b64_tr_b16 v[118:119], v154 offset:19112\n\t" \
  "ds_read_b128 v[100:103], v151 offset:18432\n\t" \
  "ds_read_b64_tr_b16 v[120:121], v154 offset:20992\n\t" \
  "v_mfma_f32_32x32x16_bf16 v[16:31], v[72:75], v[52:55], v[16:31]\n\t" \
  "ds_read_b64_tr_b16 v[122:123], v154 offset:21632\n\t" \
  "ds_read_b64_tr_b16 v[124:125], v154 offset:21032\n\t" \
  "ds_read_b64_tr_b16 v[126:127], v154 offset:21672\n\t" \
  "v_mfma_f32_32x32x16_bf16 v[32:47], v[76:79], v[52:55], v[32:47]\n\t" \
  "ds_read_b128 v[104:107], v152 offset:18432\n\t" \
  "ds_read_b64_tr_b16 v[128:129], v154 offset:23552\n\t" \
  "ds_read_b64_tr_b16 v[130:131], v154 offset:24192\n\t" \
  "ds_read_b64_tr_b16 v[132:133], v154 offset:23592\n\t" \
  "v_mfma_f32_32x32x16_bf16 v[16:31], v[80:83], v[56:59], v[16:31]\n\t" \
  "ds_read_b64_tr_b16 v[134:135], v154 offset:24232\n\t" \
  "ds_read_b128 v[108:111], v153 offset:18432\n\t" \
  "ds_read_b64_tr_b16 v[136:137], v154 offset:26112\n\t" \
  "v_mfma_f32_32x32x16_bf16 v[32:47], v[84:87], v[56:59], v[32:47]\n\t" \
  "ds_read_b64_tr_b16 v[138:139], v154 offset:26752\n\t" \
  "ds_read_b64_tr_b16 v[140:141], v154 offset:26152\n\t" \
  "ds_read_b64_tr_b16 v[142:143], v154 offset:26792\n\t" \
  "v_mfma_f32_32x32x16_bf16 v[16:31], v[88:91], v[60:63], v[16:31]\n\t" \
  "v_mfma_f32_32x32x16_bf16 v[32:47], v[92:95], v[60:63], v[32:47]\n\t" \
  "s_waitcnt lgkmcnt(0)\n\t" \
  "global_load_dwordx4 v[156:159], v155, s[76:77] sc1\n\t" \
  "v_mfma_f32_32x32x16_bf16 v[16:31], v[112:115], v[96:99], v[16:31]\n\t" \
  "v_mfma_f32_32x32x16_bf16 v[32:47], v[116:119], v[96:99], v[32:47]\n\t" \
  "v_mfma_f32_32x32x16_bf16 v[16:31], v[120:123], v[100:103], v[16:31]\n\t" \
  "v_mfma_f32_32x32x16_bf16 v[32:47], v[124:127], v[100:103], v[32:47]\n\t" \
  "v_mfma_f32_32x32x16_bf16 v[16:31], v[128:131], v[104:107], v[16:31]\n\t" \
  "v_mfma_f32_32x32x16_bf16 v[32:47], v[132:135], v[104:107], v[32:47]\n\t" \
  "v_mfma_f32_32x32x16_bf16 v[16:31], v[136:139], v[108:111], v[16:31]\n\t" \
  "v_mfma_f32_32x32x16_bf16 v[32:47], v[140:143], v[108:111], v[32:47]\n\t" \
  "s_nop 15\n\t" \
  ""
#define V10(b) "v" #b "0","v" #b "1","v" #b "2","v" #b "3","v" #b "4","v" #b "5","v" #b "6","v" #b "7","v" #b "8","v" #b "9"
#define QP_CLOB "v160","v161","v48","v49",V10(5),V10(6),V10(7),V10(8),V10(9),V10(10),V10(11),V10(12),V10(13),"v140","v141","v142","v143","scc","memory"
DI uint64_t uni64(const void* p) { const uint64_t a = (uint64_t)(uintptr_t)p; return ((uint64_t)rfl((unsigned)(a >> 32)) << 32) | rfl((unsigned)a); }
DI void q_proj(const uint16_t* x, const uint16_t* wq, const void* flags, int tt, int hg, char* smem, f32x16 (&accq)[2], u32x4& look, const void* epw, unsigned& epoch) {
    const int tid = threadIdx.x, lane = tid & 63, w = rfl(tid >> 6), r = lane & 31, h = lane >> 5;
    const unsigned lds0 = (unsigned)(uintptr_t)smem;
    unsigned vd[5], ld[5];
#pragma unroll
    for (int i = 0; i < 2; ++i) { const int row = (2 * w + i) * 8 + (lane >> 3); vd[i] = row * 1280 + (((lane & 7) ^ ((row >> 1) & 7)) << 4); ld[i] = rfl(lds0 + PX + (2 * w + i) * 1024); }
    const int wq0 = (w < 2) ? 3 * w : 6 + 2 * (w - 2);
#pragma unroll
    for (int i = 0; i < 3; ++i) { const int pc = wq0 + ((w >= 2 && i == 2) ? 1 : i), b = pc * 1024 + lane * 16; vd[2 + i] = (b / 160) * 1280 + b % 160; ld[2 + i] = rfl(lds0 + PWQ + pc * 1024); }
    const int cw = w & 1, hp = w >> 1, blk = (lane >> 4) & 1, q = (lane & 15) >> 2, p = lane & 3;
    const int xrow = cw * 32 + r, xsw = (xrow >> 1) & 7;
    unsigned vx[4];
#pragma unroll
    for (int s = 0; s < 4; ++s) vx[s] = lds0 + PX + xrow * 128 + (((2 * s + h) ^ xsw) << 4);
    const unsigned vw = lds0 + PWQ + (8 * h + q) * 160 + (hp * 40 + 16 * blk + 4 * p) * 2;
    accq[0] = f32x16{}; accq[1] = f32x16{};
    uint64_t xp = uni64(x + (size_t)tt * 64 * D), wp = uni64(wq + hg * 80);
    asm volatile(QP_ASM
        : "+{v[16:31]}"(accq[0]), "+{v[32:47]}"(accq[1]), "+{s[60:61]}"(xp), "+{s[62:63]}"(wp), "={v[156:159]}"(look), "={s80}"(epoch)
        : "{s[76:77]}"(uni64(flags)), "{s[78:79]}"(uni64(epw)), "{v144}"(vd[0]), "{v145}"(vd[1]), "{v146}"(vd[2]), "{v147}"(vd[3]), "{v148}"(vd[4]), "{v150}"(vx[0]), "{v151}"(vx[1]), "{v152}"(vx[2]), "{v153}"(vx[3]), "{v154}"(vw), "{v155}"(lane * 16),
          "{s64}"(ld[0]), "{s65}"(ld[1]), "{s66}"(ld[2]), "{s67}"(ld[3]), "{s68}"(ld[4])
        : QP_CLOB);
}
DI float dot2bf(unsigned p, float c) { const bf16x2_t v = __builtin_bit_cast(bf16x2_t, p); return __builtin_amdgcn_fdot2_f32_bf16(v, v, c, false); }
struct QF { bf16x8 q1; s16x4 q2; };
DI float halfswap_max(float m) { auto rr = __builtin_amdgcn_permlane32_swap(__float_as_uint(m), __float_as_uint(m), false, false); return fmaxf(__uint_as_float(rr[0]), __uint_as_float(rr[1])); }
DI bf16x8 as8(u32x4 v) { return __builtin_bit_cast(bf16x8, v); }
DI s16x4 as4(u32x2 v) { return __builtin_bit_cast(s16x4, v); }
DI float bf_lo(unsigned v) { return __uint_as_float(v << 16); }
DI float bf_hi(unsigned v) { return __uint_as_float(v & 0xffff0000u); }
DI void q_slow(const uint16_t* x, const uint16_t* wq, int token, int head, int h, QF& q) {
    float a[12] = {};
#pragma unroll 2
    for (int k = 0; k < D; ++k) {
        const float xv = __uint_as_float((unsigned)x[(size_t)token * D + k] << 16);
        const uint16_t* wr = wq + (size_t)k * D + head * DH;
        const u32x2 w0 = *(const u32x2*)(wr + 4 * h), w1 = *(const u32x2*)(wr + 8 + 4 * h), w2 = *(const u32x2*)(wr + 16);
        a[0] += xv * bf_lo(w0.x); a[1] += xv * bf_hi(w0.x); a[2] += xv * bf_lo(w0.y); a[3] += xv * bf_hi(w0.y);
        a[4] += xv * bf_lo(w1.x); a[5] += xv * bf_hi(w1.x); a[6] += xv * bf_lo(w1.y); a[7] += xv * bf_hi(w1.y);
        a[8] += xv * bf_lo(w2.x); a[9] += xv * bf_hi(w2.x); a[10] += xv * bf_lo(w2.y); a[11] += xv * bf_hi(w2.y);
    }
    q.q1 = as8(u32x4{cvtpk(a[0] * QSCALE, a[1] * QSCALE), cvtpk(a[2] * QSCALE, a[3] * QSCALE), cvtpk(a[4] * QSCALE, a[5] * QSCALE), cvtpk(a[6] * QSCALE, a[7] * QSCALE)});
    q.q2 = as4(u32x2{cvtpk(a[8] * QSCALE, a[9] * QSCALE), cvtpk(a[10] * QSCALE, a[11] * QSCALE)});
}
DI void attn_exact(QF (&q)[2], f32x16 (&o)[2], rsrc_t R, int lane) {
    const int h = lane >> 5;
#pragma unroll
    for (int c = 0; c < 2; ++c) if (h) q[c].q2 = s16x4{0, 0, 0, 0};
    float m[2] = {-INFINITY, -INFINITY};
    for (int t = 0; t < 64; ++t) {
        const unsigned p = WS_KV + t * KVT; const bf16x8 k1 = as8(ld16(R, p + lane * 16)); const s16x4 k2 = as4(ld8(R, p + 1024 + lane * 8));
#pragma unroll
        for (int c = 0; c < 2; ++c) { f32x16 s = MFMA16(k1, q[c].q1, f32x16{}); s = MFMA8(k2, q[c].q2, s);
#pragma unroll
            for (int i = 0; i < 16; ++i) m[c] = fmaxf(m[c], s[i]); }
    }
#pragma unroll
    for (int c = 0; c < 2; ++c) {
        const float nm = -halfswap_max(m[c]), a = bf16r(nm), b = bf16r(nm - a), d = bf16r(nm - a - b);
        if (h) q[c].q2 = as4(u32x2{cvtpk(a, b), cvtpk(d, 0.f)});
        o[c] = f32x16{};
    }
    for (int t = 0; t < 64; ++t) {
        const unsigned p = WS_KV + t * KVT; const bf16x8 k1 = as8(ld16(R, p + lane * 16)); const s16x4 k2 = as4(ld8(R, p + 1024 + lane * 8));
        const bf16x8 v0 = as8(ld16(R, p + 1536 + lane * 16)), v1 = as8(ld16(R, p + 2560 + lane * 16));
#pragma unroll
        for (int c = 0; c < 2; ++c) { f32x16 s = MFMA16(k1, q[c].q1, f32x16{}); s = MFMA8(k2, q[c].q2, s);
            unsigned pk[8];
#pragma unroll
            for (int i = 0; i < 8; ++i) pk[i] = cvtpk(__builtin_amdgcn_exp2f(s[2 * i]), __builtin_amdgcn_exp2f(s[2 * i + 1]));
            o[c] = MFMA16(v0, as8(u32x4{pk[0], pk[1], pk[2], pk[3]}), o[c]); o[c] = MFMA16(v1, as8(u32x4{pk[4], pk[5], pk[6], pk[7]}), o[c]); }
    }
}
#define ATTN_ASM \
  "v_mov_b32 v44, 0\n\t" \
  "v_mov_b32 v72, 0\n\t" \
  "v_mov_b32 v45, 0\n\t" \
  "v_mov_b32 v73, 0\n\t" \
  "v_mov_b32 v46, 0\n\t" \
  "v_mov_b32 v74, 0\n\t" \
  "v_mov_b32 v47, 0\n\t" \
  "v_mov_b32 v75, 0\n\t" \
  "global_load_dword v78, v79, s[64:65] sc1\n\t" \
  "global_load_dwordx4 v[48:51], v76, s[60:61] sc1\n\t" \
  "global_load_dwordx2 v[56:57], v77, s[60:61] offset:1024 sc1\n\t" \
  "global_load_dwordx4 v[60:63], v76, s[60:61] offset:1536 sc1\n\t" \
  "global_load_dwordx4 v[64:67], v76, s[60:61] offset:2560 sc1\n\t" \
  "s_add_u32 s60, s60, 3584\n\t" \
  "s_addc_u32 s61, s61, 0\n\t" \
  "v_mfma_f32_32x32x16_bf16 v[128:143], v[72:75], v[44:47], 0\n\t" \
  "v_mfma_f32_32x32x16_bf16 v[144:159], v[72:75], v[44:47], 0\n\t" \
  "v_mfma_f32_32x32x16_bf16 v[160:175], v[72:75], v[44:47], 0\n\t" \
  "v_mfma_f32_32x32x16_bf16 v[176:191], v[72:75], v[44:47], 0\n\t" \
  "v_mfma_f32_32x32x16_bf16 v[192:207], v[72:75], v[44:47], 0\n\t" \
  "v_mfma_f32_32x32x16_bf16 v[208:223], v[72:75], v[44:47], 0\n\t" \
  "v_mfma_f32_32x32x16_bf16 v[224:239], v[72:75], v[44:47], 0\n\t" \
  "v_mfma_f32_32x32x16_bf16 v[240:255], v[72:75], v[44:47], 0\n\t" \
  "s_mov_b32 s62, 0\n\t" \
  "s_waitcnt vmcnt(2)\n\t" \
  "v_mfma_f32_32x32x16_bf16 v[8:23], v[48:51], v[80:83], 0\n\t" \
  "v_mfma_f32_32x32x8_bf16 v[8:23], v[56:57], v[84:85], v[8:23]\n\t" \
  "s_nop 15\n\t" \
  "ATTN_LOOP%=:\n\t" \
  "s_waitcnt vmcnt(0)\n\t" \
  "v_exp_f32 v8, v8\n\t" \
  "v_exp_f32 v9, v9\n\t" \
  "v_mfma_f32_32x32x16_bf16 v[240:255], v[72:75], v[44:47], v[240:255]\n\t" \
  "global_load_dwordx4 v[52:55], v76, s[60:61] sc1\n\t" \
  "global_load_dwordx2 v[58:59], v77, s[60:61] offset:1024 sc1\n\t" \
  "global_load_dwordx4 v[68:71], v76, s[60:61] offset:1536 sc1\n\t" \
  "global_load_dwordx4 v[72:75], v76, s[60:61] offset:2560 sc1\n\t" \
  "s_add_u32 s60, s60, 3584\n\t" \
  "s_addc_u32 s61, s61, 0\n\t" \
  "s_cmp_eq_u32 s62, 0\n\t" \
  "s_cbranch_scc0 ATTN_NOFLAG%=\n\t" \
  "s_mov_b64 exec, 1\n\t" \
  "global_store_dwordx2 v6, v[4:5], s[66:67] sc1\n\t" \
  "s_mov_b64 exec, -1\n\t" \
  "ATTN_NOFLAG%=:\n\t" \
  "v_exp_f32 v10, v10\n\t" \
  "v_exp_f32 v11, v11\n\t" \
  "v_cvt_pk_bf16_f32 v40, v8, v9\n\t" \
  "v_cvt_pk_bf16_f32 v41, v10, v11\n\t" \
  "v_mfma_f32_32x32x16_bf16 v[24:39], v[48:51], v[86:89], 0\n\t" \
  "v_exp_f32 v12, v12\n\t" \
  "v_exp_f32 v13, v13\n\t" \
  "v_exp_f32 v14, v14\n\t" \
  "v_exp_f32 v15, v15\n\t" \
  "v_cvt_pk_bf16_f32 v42, v12, v13\n\t" \
  "v_cvt_pk_bf16_f32 v43, v14, v15\n\t" \
  "v_mfma_f32_32x32x8_bf16 v[24:39], v[56:57], v[90:91], v[24:39]\n\t" \
  "v_exp_f32 v16, v16\n\t" \
  "v_exp_f32 v17, v17\n\t" \
  "v_exp_f32 v18, v18\n\t" \
  "v_exp_f32 v19, v19\n\t" \
  "v_cvt_pk_bf16_f32 v44, v16, v17\n\t" \
  "v_cvt_pk_bf16_f32 v45, v18, v19\n\t" \
  "v_mfma_f32_32x32x16_bf16 v[128:143], v[60:63], v[40:43], v[128:143]\n\t" \
  "v_exp_f32 v20, v20\n\t" \
  "v_exp_f32 v21, v21\n\t" \
  "v_exp_f32 v22, v22\n\t" \
  "v_exp_f32 v23, v23\n\t" \
  "v_cvt_pk_bf16_f32 v46, v20, v21\n\t" \
  "v_cvt_pk_bf16_f32 v47, v22, v23\n\t" \
  "v_exp_f32 v24, v24\n\t" \
  "v_exp_f32 v25, v25\n\t" \
  "v_mfma_f32_32x32x16_bf16 v[128:143], v[64:67], v[44:47], v[128:143]\n\t" \
  "v_exp_f32 v26, v26\n\t" \
  "v_exp_f32 v27, v27\n\t" \
  "v_cvt_pk_bf16_f32 v40, v24, v25\n\t" \
  "v_cvt_pk_bf16_f32 v41, v26, v27\n\t" \
  "v_mfma_f32_32x32x16_bf16 v[8:23], v[48:51], v[92:95], 0\n\t" \
  "v_exp_f32 v28, v28\n\t" \
  "v_exp_f32 v29, v29\n\t" \
  "v_exp_f32 v30, v30\n\t" \
  "v_exp_f32 v31, v31\n\t" \
  "v_cvt_pk_bf16_f32 v42, v28, v29\n\t" \
  "v_cvt_pk_bf16_f32 v43, v30, v31\n\t" \
  "v_mfma_f32_32x32x8_bf16 v[8:23], v[56:57], v[96:97], v[8:23]\n\t" \
  "v_exp_f32 v32, v32\n\t" \
  "v_exp_f32 v33, v33\n\t" \
  "v_exp_f32 v34, v34\n\t" \
  "v_exp_f32 v35, v35\n\t" \
  "v_cvt_pk_bf16_f32 v44, v32, v33\n\t" \
  "v_cvt_pk_bf16_f32 v45, v34, v35\n\t" \
  "v_mfma_f32_32x32x16_bf16 v[144:159], v[60:63], v[40:43], v[144:159]\n\t" \
  "v_exp_f32 v36, v36\n\t" \
  "v_exp_f32 v37, v37\n\t" \
  "v_exp_f32 v38, v38\n\t" \
  "v_exp_f32 v39, v39\n\t" \
  "v_cvt_pk_bf16_f32 v46, v36, v37\n\t" \
  "v_cvt_pk_bf16_f32 v47, v38, v39\n\t" \
  "v_exp_f32 v8, v8\n\t" \
  "v_exp_f32 v9, v9\n\t" \
  "v_mfma_f32_32x32x16_bf16 v[144:159], v[64:67], v[44:47], v[144:159]\n\t" \
  "v_exp_f32 v10, v10\n\t" \
  "v_exp_f32 v11, v11\n\t" \
  "v_cvt_pk_bf16_f32 v40, v8, v9\n\t" \
  "v_cvt_pk_bf16_f32 v41, v10, v11\n\t" \
  "v_mfma_f32_32x32x16_bf16 v[24:39], v[48:51], v[98:101], 0\n\t" \
  "v_exp_f32 v12, v12\n\t" \
  "v_exp_f32 v13, v13\n\t" \
  "v_exp_f32 v14, v14\n\t" \
  "v_exp_f32 v15, v15\n\t" \
  "v_cvt_pk_bf16_f32 v42, v12, v13\n\t" \
  "v_cvt_pk_bf16_f32 v43, v14, v15\n\t" \
  "v_mfma_f32_32x32x8_bf16 v[24:39], v[56:57], v[102:103], v[24:39]\n\t" \
  "v_exp_f32 v16, v16\n\t" \
  "v_exp_f32 v17, v17\n\t" \
  "v_exp_f32 v18, v18\n\t" \
  "v_exp_f32 v19, v19\n\t" \
  "v_cvt_pk_bf16_f32 v44, v16, v17\n\t" \
  "v_cvt_pk_bf16_f32 v45, v18, v19\n\t" \
  "v_mfma_f32_32x32x16_bf16 v[160:175], v[60:63], v[40:43], v[160:175]\n\t" \
  "v_exp_f32 v20, v20\n\t" \
  "v_exp_f32 v21, v21\n\t" \
  "v_exp_f32 v22, v22\n\t" \
  "v_exp_f32 v23, v23\n\t" \
  "v_cvt_pk_bf16_f32 v46, v20, v21\n\t" \
  "v_cvt_pk_bf16_f32 v47, v22, v23\n\t" \
  "v_exp_f32 v24, v24\n\t" \
  "v_exp_f32 v25, v25\n\t" \
  "v_mfma_f32_32x32x16_bf16 v[160:175], v[64:67], v[44:47], v[160:175]\n\t" \
  "v_exp_f32 v26, v26\n\t" \
  "v_exp_f32 v27, v27\n\t" \
  "v_cvt_pk_bf16_f32 v40, v24, v25\n\t" \
  "v_cvt_pk_bf16_f32 v41, v26, v27\n\t" \
  "v_mfma_f32_32x32x16_bf16 v[8:23], v[48:51], v[104:107], 0\n\t" \
  "v_exp_f32 v28, v28\n\t" \
  "v_exp_f32 v29, v29\n\t" \
  "v_exp_f32 v30, v30\n\t" \
  "v_exp_f32 v31, v31\n\t" \
  "v_cvt_pk_bf16_f32 v42, v28, v29\n\t" \
  "v_cvt_pk_bf16_f32 v43, v30, v31\n\t" \
  "v_mfma_f32_32x32x8_bf16 v[8:23], v[56:57], v[108:109], v[8:23]\n\t" \
  "v_exp_f32 v32, v32\n\t" \
  "v_exp_f32 v33, v33\n\t" \
  "v_exp_f32 v34, v34\n\t" \
  "v_exp_f32 v35, v35\n\t" \
  "v_cvt_pk_bf16_f32 v44, v32, v33\n\t" \
  "v_cvt_pk_bf16_f32 v45, v34, v35\n\t" \
  "v_mfma_f32_32x32x16_bf16 v[176:191], v[60:63], v[40:43], v[176:191]\n\t" \
  "v_exp_f32 v36, v36\n\t" \
  "v_exp_f32 v37, v37\n\t" \
  "v_exp_f32 v38, v38\n\t" \
  "v_exp_f32 v39, v39\n\t" \
  "v_cvt_pk_bf16_f32 v46, v36, v37\n\t" \
  "v_cvt_pk_bf16_f32 v47, v38, v39\n\t" \
  "v_exp_f32 v8, v8\n\t" \
  "v_exp_f32 v9, v9\n\t" \
  "v_mfma_f32_32x32x16_bf16 v[176:191], v[64:67], v[44:47], v[176:191]\n\t" \
  "v_exp_f32 v10, v10\n\t" \
  "v_exp_f32 v11, v11\n\t" \
  "v_cvt_pk_bf16_f32 v40, v8, v9\n\t" \
  "v_cvt_pk_bf16_f32 v41, v10, v11\n\t" \
  "v_mfma_f32_32x32x16_bf16 v[24:39], v[48:51], v[110:113], 0\n\t" \
  "v_exp_f32 v12, v12\n\t" \
  "v_exp_f32 v13, v13\n\t" \
  "v_exp_f32 v14, v14\n\t" \
  "v_exp_f32 v15, v15\n\t" \
  "v_cvt_pk_bf16_f32 v42, v12, v13\n\t" \
  "v_cvt_pk_bf16_f32 v43, v14, v15\n\t" \
  "v_mfma_f32_32x32x8_bf16 v[24:39], v[56:57], v[114:115], v[24:39]\n\t" \
  "v_exp_f32 v16, v16\n\t" \
  "v_exp_f32 v17, v17\n\t" \
  "v_exp_f32 v18, v18\n\t" \
  "v_exp_f32 v19, v19\n\t" \
  "v_cvt_pk_bf16_f32 v44, v16, v17\n\t" \
  "v_cvt_pk_bf16_f32 v45, v18, v19\n\t" \
  "v_mfma_f32_32x32x16_bf16 v[192:207], v[60:63], v[40:43], v[192:207]\n\t" \
  "v_exp_f32 v20, v20\n\t" \
  "v_exp_f32 v21, v21\n\t" \
  "v_exp_f32 v22, v22\n\t" \
  "v_exp_f32 v23, v23\n\t" \
  "v_cvt_pk_bf16_f32 v46, v20, v21\n\t" \
  "v_cvt_pk_bf16_f32 v47, v22, v23\n\t" \
  "v_exp_f32 v24, v24\n\t" \
  "v_exp_f32 v25, v25\n\t" \
  "v_mfma_f32_32x32x16_bf16 v[192:207], v[64:67], v[44:47], v[192:207]\n\t" \
  "v_exp_f32 v26, v26\n\t" \
  "v_exp_f32 v27, v27\n\t" \
  "v_cvt_pk_bf16_f32 v40, v24, v25\n\t" \
  "v_cvt_pk_bf16_f32 v41, v26, v27\n\t" \
  "v_mfma_f32_32x32x16_bf16 v[8:23], v[48:51], v[116:119], 0\n\t" \
  "v_exp_f32 v28, v28\n\t" \
  "v_exp_f32 v29, v29\n\t" \
  "v_exp_f32 v30, v30\n\t" \
  "v_exp_f32 v31, v31\n\t" \
  "v_cvt_pk_bf16_f32 v42, v28, v29\n\t" \
  "v_cvt_pk_bf16_f32 v43, v30, v31\n\t" \
  "v_mfma_f32_32x32x8_bf16 v[8:23], v[56:57], v[120:121], v[8:23]\n\t" \
  "v_exp_f32 v32, v32\n\t" \
  "v_exp_f32 v33, v33\n\t" \
  "v_exp_f32 v34, v34\n\t" \
  "v_exp_f32 v35, v35\n\t" \
  "v_cvt_pk_bf16_f32 v44, v32, v33\n\t" \
  "v_cvt_pk_bf16_f32 v45, v34, v35\n\t" \
  "v_mfma_f32_32x32x16_bf16 v[208:223], v[60:63], v[40:43], v[208:223]\n\t" \
  "v_exp_f32 v36, v36\n\t" \
  "v_exp_f32 v37, v37\n\t" \
  "v_exp_f32 v38, v38\n\t" \
  "v_exp_f32 v39, v39\n\t" \
  "v_cvt_pk_bf16_f32 v46, v36, v37\n\t" \
  "v_cvt_pk_bf16_f32 v47, v38, v39\n\t" \
  "v_exp_f32 v8, v8\n\t" \
  "v_exp_f32 v9, v9\n\t" \
  "v_mfma_f32_32x32x16_bf16 v[208:223], v[64:67], v[44:47], v[208:223]\n\t" \
  "v_exp_f32 v10, v10\n\t" \
  "v_exp_f32 v11, v11\n\t" \
  "v_cvt_pk_bf16_f32 v40, v8, v9\n\t" \
  "v_cvt_pk_bf16_f32 v41, v10, v11\n\t" \
  "v_mfma_f32_32x32x16_bf16 v[24:39], v[48:51], v[122:125], 0\n\t" \
  "v_exp_f32 v12, v12\n\t" \
  "v_exp_f32 v13, v13\n\t" \
  "v_exp_f32 v14, v14\n\t" \
  "v_exp_f32 v15, v15\n\t" \
  "v_cvt_pk_bf16_f32 v42, v12, v13\n\t" \
  "v_cvt_pk_bf16_f32 v43, v14, v15\n\t" \
  "v_mfma_f32_32x32x8_bf16 v[24:39], v[56:57], v[126:127], v[24:39]\n\t" \
  "v_exp_f32 v16, v16\n\t" \
  "v_exp_f32 v17, v17\n\t" \
  "v_exp_f32 v18, v18\n\t" \
  "v_exp_f32 v19, v19\n\t" \
  "v_cvt_pk_bf16_f32 v44, v16, v17\n\t" \
  "v_cvt_pk_bf16_f32 v45, v18, v19\n\t" \
  "v_mfma_f32_32x32x16_bf16 v[224:239], v[60:63], v[40:43], v[224:239]\n\t" \
  "v_exp_f32 v20, v20\n\t" \
  "v_exp_f32 v21, v21\n\t" \
  "v_exp_f32 v22, v22\n\t" \
  "v_exp_f32 v23, v23\n\t" \
  "v_cvt_pk_bf16_f32 v46, v20, v21\n\t" \
  "v_cvt_pk_bf16_f32 v47, v22, v23\n\t" \
  "v_exp_f32 v24, v24\n\t" \
  "v_exp_f32 v25, v25\n\t" \
  "v_mfma_f32_32x32x16_bf16 v[224:239], v[64:67], v[44:47], v[224:239]\n\t" \
  "v_exp_f32 v26, v26\n\t" \
  "v_exp_f32 v27, v27\n\t" \
  "v_cvt_pk_bf16_f32 v40, v24, v25\n\t" \
  "v_cvt_pk_bf16_f32 v41, v26, v27\n\t" \
  "s_waitcnt vmcnt(2)\n\t" \
  "v_mfma_f32_32x32x16_bf16 v[8:23], v[52:55], v[80:83], 0\n\t" \
  "v_exp_f32 v28, v28\n\t" \
  "v_exp_f32 v29, v29\n\t" \
  "v_exp_f32 v30, v30\n\t" \
  "v_exp_f32 v31, v31\n\t" \
  "v_cvt_pk_bf16_f32 v42, v28, v29\n\t" \
  "v_cvt_pk_bf16_f32 v43, v30, v31\n\t" \
  "v_mfma_f32_32x32x8_bf16 v[8:23], v[58:59], v[84:85], v[8:23]\n\t" \
  "v_exp_f32 v32, v32\n\t" \
  "v_exp_f32 v33, v33\n\t" \
  "v_exp_f32 v34, v34\n\t" \
  "v_exp_f32 v35, v35\n\t" \
  "v_cvt_pk_bf16_f32 v44, v32, v33\n\t" \
  "v_cvt_pk_bf16_f32 v45, v34, v35\n\t" \
  "v_mfma_f32_32x32x16_bf16 v[240:255], v[60:63], v[40:43], v[240:255]\n\t" \
  "v_exp_f32 v36, v36\n\t" \
  "v_exp_f32 v37, v37\n\t" \
  "v_exp_f32 v38, v38\n\t" \
  "v_exp_f32 v39, v39\n\t" \
  "v_cvt_pk_bf16_f32 v46, v36, v37\n\t" \
  "v_cvt_pk_bf16_f32 v47, v38, v39\n\t" \
  "s_waitcnt vmcnt(0)\n\t" \
  "v_exp_f32 v8, v8\n\t" \
  "v_exp_f32 v9, v9\n\t" \
  "v_mfma_f32_32x32x16_bf16 v[240:255], v[64:67], v[44:47], v[240:255]\n\t" \
  "global_load_dwordx4 v[48:51], v76, s[60:61] sc1\n\t" \
  "global_load_dwordx2 v[56:57], v77, s[60:61] offset:1024 sc1\n\t" \
  "global_load_dwordx4 v[60:63], v76, s[60:61] offset:1536 sc1\n\t" \
  "global_load_dwordx4 v[64:67], v76, s[60:61] offset:2560 sc1\n\t" \
  "s_add_u32 s60, s60, 3584\n\t" \
  "s_addc_u32 s61, s61, 0\n\t" \
  "v_exp_f32 v10, v10\n\t" \
  "v_exp_f32 v11, v11\n\t" \
  "v_cvt_pk_bf16_f32 v40, v8, v9\n\t" \
  "v_cvt_pk_bf16_f32 v41, v10, v11\n\t" \
  "v_mfma_f32_32x32x16_bf16 v[24:39], v[52:55], v[86:89], 0\n\t" \
  "v_exp_f32 v12, v12\n\t" \
  "v_exp_f32 v13, v13\n\t" \
  "v_exp_f32 v14, v14\n\t" \
  "v_exp_f32 v15, v15\n\t" \
  "v_cvt_pk_bf16_f32 v42, v12, v13\n\t" \
  "v_cvt_pk_bf16_f32 v43, v14, v15\n\t" \
  "v_mfma_f32_32x32x8_bf16 v[24:39], v[58:59], v[90:91], v[24:39]\n\t" \
  "v_exp_f32 v16, v16\n\t" \
  "v_exp_f32 v17, v17\n\t" \
  "v_exp_f32 v18, v18\n\t" \
  "v_exp_f32 v19, v19\n\t" \
  "v_cvt_pk_bf16_f32 v44, v16, v17\n\t" \
  "v_cvt_pk_bf16_f32 v45, v18, v19\n\t" \
  "v_mfma_f32_32x32x16_bf16 v[128:143], v[68:71], v[40:43], v[128:143]\n\t" \
  "v_exp_f32 v20, v20\n\t" \
  "v_exp_f32 v21, v21\n\t" \
  "v_exp_f32 v22, v22\n\t" \
  "v_exp_f32 v23, v23\n\t" \
  "v_cvt_pk_bf16_f32 v46, v20, v21\n\t" \
  "v_cvt_pk_bf16_f32 v47, v22, v23\n\t" \
  "v_exp_f32 v24, v24\n\t" \
  "v_exp_f32 v25, v25\n\t" \
  "v_mfma_f32_32x32x16_bf16 v[128:143], v[72:75], v[44:47], v[128:143]\n\t" \
  "v_exp_f32 v26, v26\n\t" \
  "v_exp_f32 v27, v27\n\t" \
  "v_cvt_pk_bf16_f32 v40, v24, v25\n\t" \
  "v_cvt_pk_bf16_f32 v41, v26, v27\n\t" \
  "v_mfma_f32_32x32x16_bf16 v[8:23], v[52:55], v[92:95], 0\n\t" \
  "v_exp_f32 v28, v28\n\t" \
  "v_exp_f32 v29, v29\n\t" \
  "v_exp_f32 v30, v30\n\t" \
  "v_exp_f32 v31, v31\n\t" \
  "v_cvt_pk_bf16_f32 v42, v28, v29\n\t" \
  "v_cvt_pk_bf16_f32 v43, v30, v31\n\t" \
  "v_mfma_f32_32x32x8_bf16 v[8:23], v[58:59], v[96:97], v[8:23]\n\t" \
  "v_exp_f32 v32, v32\n\t" \
  "v_exp_f32 v33, v33\n\t" \
  "v_exp_f32 v34, v34\n\t" \
  "v_exp_f32 v35, v35\n\t" \
  "v_cvt_pk_bf16_f32 v44, v32, v33\n\t" \
  "v_cvt_pk_bf16_f32 v45, v34, v35\n\t" \
  "v_mfma_f32_32x32x16_bf16 v[144:159], v[68:71], v[40:43], v[144:159]\n\t" \
  "v_exp_f32 v36, v36\n\t" \
  "v_exp_f32 v37, v37\n\t" \
  "v_exp_f32 v38, v38\n\t" \
  "v_exp_f32 v39, v39\n\t" \
  "v_cvt_pk_bf16_f32 v46, v36, v37\n\t" \
  "v_cvt_pk_bf16_f32 v47, v38, v39\n\t" \
  "v_exp_f32 v8, v8\n\t" \
  "v_exp_f32 v9, v9\n\t" \
  "v_mfma_f32_32x32x16_bf16 v[144:159], v[72:75], v[44:47], v[144:159]\n\t" \
  "v_exp_f32 v10, v10\n\t" \
  "v_exp_f32 v11, v11\n\t" \
  "v_cvt_pk_bf16_f32 v40, v8, v9\n\t" \
  "v_cvt_pk_bf16_f32 v41, v10, v11\n\t" \
  "v_mfma_f32_32x32x16_bf16 v[24:39], v[52:55], v[98:101], 0\n\t" \
  "v_exp_f32 v12, v12\n\t" \
  "v_exp_f32 v13, v13\n\t" \
  "v_exp_f32 v14, v14\n\t" \
  "v_exp_f32 v15, v15\n\t" \
  "v_cvt_pk_bf16_f32 v42, v12, v13\n\t" \
  "v_cvt_pk_bf16_f32 v43, v14, v15\n\t" \
  "v_mfma_f32_32x32x8_bf16 v[24:39], v[58:59], v[102:103], v[24:39]\n\t" \
  "v_exp_f32 v16, v16\n\t" \
  "v_exp_f32 v17, v17\n\t" \
  "v_exp_f32 v18, v18\n\t" \
  "v_exp_f32 v19, v19\n\t" \
  "v_cvt_pk_bf16_f32 v44, v16, v17\n\t" \
  "v_cvt_pk_bf16_f32 v45, v18, v19\n\t" \
  "v_mfma_f32_32x32x16_bf16 v[160:175], v[68:71], v[40:43], v[160:175]\n\t" \
  "v_exp_f32 v20, v20\n\t" \
  "v_exp_f32 v21, v21\n\t" \
  "v_exp_f32 v22, v22\n\t" \
  "v_exp_f32 v23, v23\n\t" \
  "v_cvt_pk_bf16_f32 v46, v20, v21\n\t" \
  "v_cvt_pk_bf16_f32 v47, v22, v23\n\t" \
  "v_exp_f32 v24, v24\n\t" \
  "v_exp_f32 v25, v25\n\t" \
  "v_mfma_f32_32x32x16_bf16 v[160:175], v[72:75], v[44:47], v[160:175]\n\t" \
  "v_exp_f32 v26, v26\n\t" \
  "v_exp_f32 v27, v27\n\t" \
  "v_cvt_pk_bf16_f32 v40, v24, v25\n\t" \
  "v_cvt_pk_bf16_f32 v41, v26, v27\n\t" \
  "v_mfma_f32_32x32x16_bf16 v[8:23], v[52:55], v[104:107], 0\n\t" \
  "v_exp_f32 v28, v28\n\t" \
  "v_exp_f32 v29, v29\n\t" \
  "v_exp_f32 v30, v30\n\t" \
  "v_exp_f32 v31, v31\n\t" \
  "v_cvt_pk_bf16_f32 v42, v28, v29\n\t" \
  "v_cvt_pk_bf16_f32 v43, v30, v31\n\t" \
  "v_mfma_f32_32x32x8_bf16 v[8:23], v[58:59], v[108:109], v[8:23]\n\t" \
  "v_exp_f32 v32, v32\n\t" \
  "v_exp_f32 v33, v33\n\t" \
  "v_exp_f32 v34, v34\n\t" \
  "v_exp_f32 v35, v35\n\t" \
  "v_cvt_pk_bf16_f32 v44, v32, v33\n\t" \
  "v_cvt_pk_bf16_f32 v45, v34, v35\n\t" \
  "v_mfma_f32_32x32x16_bf16 v[176:191], v[68:71], v[40:43], v[176:191]\n\t" \
  "v_exp_f32 v36, v36\n\t" \
  "v_exp_f32 v37, v37\n\t" \
  "v_exp_f32 v38, v38\n\t" \
  "v_exp_f32 v39, v39\n\t" \
  "v_cvt_pk_bf16_f32 v46, v36, v37\n\t" \
  "v_cvt_pk_bf16_f32 v47, v38, v39\n\t" \
  "v_exp_f32 v8, v8\n\t" \
  "v_exp_f32 v9, v9\n\t" \
  "v_mfma_f32_32x32x16_bf16 v[176:191], v[72:75], v[44:47], v[176:191]\n\t" \
  "v_exp_f32 v10, v10\n\t" \
  "v_exp_f32 v11, v11\n\t" \
  "v_cvt_pk_bf16_f32 v40, v8, v9\n\t" \
  "v_cvt_pk_bf16_f32 v41, v10, v11\n\t" \
  "v_mfma_f32_32x32x16_bf16 v[24:39], v[52:55], v[110:113], 0\n\t" \
  "v_exp_f32 v12, v12\n\t" \
  "v_exp_f32 v13, v13\n\t" \
  "v_exp_f32 v14, v14\n\t" \
  "v_exp_f32 v15, v15\n\t" \
  "v_cvt_pk_bf16_f32 v42, v12, v13\n\t" \
  "v_cvt_pk_bf16_f32 v43, v14, v15\n\t" \
  "v_mfma_f32_32x32x8_bf16 v[24:39], v[58:59], v[114:115], v[24:39]\n\t" \
  "v_exp_f32 v16, v16\n\t" \
  "v_exp_f32 v17, v17\n\t" \
  "v_exp_f32 v18, v18\n\t" \
  "v_exp_f32 v19, v19\n\t" \
  "v_cvt_pk_bf16_f32 v44, v16, v17\n\t" \
  "v_cvt_pk_bf16_f32 v45, v18, v19\n\t" \
  "v_mfma_f32_32x32x16_bf16 v[192:207], v[68:71], v[40:43], v[192:207]\n\t" \
  "v_exp_f32 v20, v20\n\t" \
  "v_exp_f32 v21, v21\n\t" \
  "v_exp_f32 v22, v22\n\t" \
  "v_exp_f32 v23, v23\n\t" \
  "v_cvt_pk_bf16_f32 v46, v20, v21\n\t" \
  "v_cvt_pk_bf16_f32 v47, v22, v23\n\t" \
  "v_exp_f32 v24, v24\n\t" \
  "v_exp_f32 v25, v25\n\t" \
  "v_mfma_f32_32x32x16_bf16 v[192:207], v[72:75], v[44:47], v[192:207]\n\t" \
  "v_exp_f32 v26, v26\n\t" \
  "v_exp_f32 v27, v27\n\t" \
  "v_cvt_pk_bf16_f32 v40, v24, v25\n\t" \
  "v_cvt_pk_bf16_f32 v41, v26, v27\n\t" \
  "v_mfma_f32_32x32x16_bf16 v[8:23], v[52:55], v[116:119], 0\n\t" \
  "v_exp_f32 v28, v28\n\t" \
  "v_exp_f32 v29, v29\n\t" \
  "v_exp_f32 v30, v30\n\t" \
  "v_exp_f32 v31, v31\n\t" \
  "v_cvt_pk_bf16_f32 v42, v28, v29\n\t" \
  "v_cvt_pk_bf16_f32 v43, v30, v31\n\t" \
  "v_mfma_f32_32x32x8_bf16 v[8:23], v[58:59], v[120:121], v[8:23]\n\t" \
  "v_exp_f32 v32, v32\n\t" \
  "v_exp_f32 v33, v33\n\t" \
  "v_exp_f32 v34, v34\n\t" \
  "v_exp_f32 v35, v35\n\t" \
  "v_cvt_pk_bf16_f32 v44, v32, v33\n\t" \
  "v_cvt_pk_bf16_f32 v45, v34, v35\n\t" \
  "v_mfma_f32_32x32x16_bf16 v[208:223], v[68:71], v[40:43], v[208:223]\n\t" \
  "v_exp_f32 v36, v36\n\t" \
  "v_exp_f32 v37, v37\n\t" \
  "v_exp_f32 v38, v38\n\t" \
  "v_exp_f32 v39, v39\n\t" \
  "v_cvt_pk_bf16_f32 v46, v36, v37\n\t" \
  "v_cvt_pk_bf16_f32 v47, v38, v39\n\t" \
  "v_exp_f32 v8, v8\n\t" \
  "v_exp_f32 v9, v9\n\t" \
  "v_mfma_f32_32x32x16_bf16 v[208:223], v[72:75], v[44:47], v[208:223]\n\t" \
  "v_exp_f32 v10, v10\n\t" \
  "v_exp_f32 v11, v11\n\t" \
  "v_cvt_pk_bf16_f32 v40, v8, v9\n\t" \
  "v_cvt_pk_bf16_f32 v41, v10, v11\n\t" \
  "v_mfma_f32_32x32x16_bf16 v[24:39], v[52:55], v[122:125], 0\n\t" \
  "v_exp_f32 v12, v12\n\t" \
  "v_exp_f32 v13, v13\n\t" \
  "v_exp_f32 v14, v14\n\t" \
  "v_exp_f32 v15, v15\n\t" \
  "v_cvt_pk_bf16_f32 v42, v12, v13\n\t" \
  "v_cvt_pk_bf16_f32 v43, v14, v15\n\t" \
  "v_mfma_f32_32x32x8_bf16 v[24:39], v[58:59], v[126:127], v[24:39]\n\t" \
  "v_exp_f32 v16, v16\n\t" \
  "v_exp_f32 v17, v17\n\t" \
  "v_exp_f32 v18, v18\n\t" \
  "v_exp_f32 v19, v19\n\t" \
  "v_cvt_pk_bf16_f32 v44, v16, v17\n\t" \
  "v_cvt_pk_bf16_f32 v45, v18, v19\n\t" \
  "v_mfma_f32_32x32x16_bf16 v[224:239], v[68:71], v[40:43], v[224:239]\n\t" \
  "v_exp_f32 v20, v20\n\t" \
  "v_exp_f32 v21, v21\n\t" \
  "v_exp_f32 v22, v22\n\t" \
  "v_exp_f32 v23, v23\n\t" \
  "v_cvt_pk_bf16_f32 v46, v20, v21\n\t" \
  "v_cvt_pk_bf16_f32 v47, v22, v23\n\t" \
  "v_exp_f32 v24, v24\n\t" \
  "v_exp_f32 v25, v25\n\t" \
  "v_mfma_f32_32x32x16_bf16 v[224:239], v[72:75], v[44:47], v[224:239]\n\t" \
  "v_exp_f32 v26, v26\n\t" \
  "v_exp_f32 v27, v27\n\t" \
  "v_cvt_pk_bf16_f32 v40, v24, v25\n\t" \
  "v_cvt_pk_bf16_f32 v41, v26, v27\n\t" \
  "s_waitcnt vmcnt(2)\n\t" \
  "v_mfma_f32_32x32x16_bf16 v[8:23], v[48:51], v[80:83], 0\n\t" \
  "v_exp_f32 v28, v28\n\t" \
  "v_exp_f32 v29, v29\n\t" \
  "v_exp_f32 v30, v30\n\t" \
  "v_exp_f32 v31, v31\n\t" \
  "v_cvt_pk_bf16_f32 v42, v28, v29\n\t" \
  "v_cvt_pk_bf16_f32 v43, v30, v31\n\t" \
  "v_mfma_f32_32x32x8_bf16 v[8:23], v[56:57], v[84:85], v[8:23]\n\t" \
  "v_exp_f32 v32, v32\n\t" \
  "v_exp_f32 v33, v33\n\t" \
  "v_exp_f32 v34, v34\n\t" \
  "v_exp_f32 v35, v35\n\t" \
  "v_cvt_pk_bf16_f32 v44, v32, v33\n\t" \
  "v_cvt_pk_bf16_f32 v45, v34, v35\n\t" \
  "v_mfma_f32_32x32x16_bf16 v[240:255], v[68:71], v[40:43], v[240:255]\n\t" \
  "v_exp_f32 v36, v36\n\t" \
  "v_exp_f32 v37, v37\n\t" \
  "v_exp_f32 v38, v38\n\t" \
  "v_exp_f32 v39, v39\n\t" \
  "v_cvt_pk_bf16_f32 v46, v36, v37\n\t" \
  "v_cvt_pk_bf16_f32 v47, v38, v39\n\t" \
  "s_add_u32 s62, s62, 1\n\t" \
  "s_cmp_lt_u32 s62, 8\n\t" \
  "s_cbranch_scc1 ATTN_LOOP%=\n\t" \
  "s_nop 1\n\t" \
  "v_mfma_f32_32x32x16_bf16 v[240:255], v[72:75], v[44:47], v[240:255]\n\t" \
  "s_waitcnt vmcnt(0)\n\t" \
  "s_nop 15\n\t" \
  ""
#define ATTN_CLOB "v8","v9",V10(1),V10(2),V10(3),V10(4),V10(5),V10(6),"v70","v71","v72","v73","v74","v75","s62","scc","memory"
template <int W> DI void merge_own(f32x16 (&o)[8], f32x16 (&mine)[2], lds_ptr L, int lane) {
#pragma unroll
    for (int a = 0; a < 2; ++a) {
        mine[a] = o[2 * W + a];
#pragma unroll
        for (int g = 0; g < 3; ++g) {
            f32x4 v = {mine[a][4 * g], mine[a][4 * g + 1], mine[a][4 * g + 2], mine[a][4 * g + 3]};
#pragma unroll
            for (int sl = 0; sl < 3; ++sl) v += *(LAS f32x4*)(L + (((W * 3 + sl) * 2 + a) * 3 + g) * 1024 + lane * 16);
            mine[a][4 * g] = v[0]; mine[a][4 * g + 1] = v[1]; mine[a][4 * g + 2] = v[2]; mine[a][4 * g + 3] = v[3];
        }
    }
}
DI unsigned make_tag(unsigned ep) { return rfl(ep) + 1u; }
DI void role_attn(const uint16_t* x, const uint16_t* wq, char* ws, int b, char* smem) {
    const int tt = (b & 7) * 4 + (b >> 6), hg = (b >> 3) & 7, tid = threadIdx.x, w = rfl(tid >> 6), cw = w & 1, hp = w >> 1;
    int lane = tid & 63; lds_ptr L = (lds_ptr)smem;
    PRIO(AP_Q);
    const rsrc_t R = ws_rsrc(ws);
    {
        const u32x4 poison = {0xffffffffu, 0xffffffffu, 0xffffffffu, 0xffffffffu};
#pragma unroll
        for (int i = 0; i < 3; ++i) { const int in = w + 4 * i;
            if (in < 10) { const int sg = 4 * in + (lane >> 4), pi = hg * 10 + sg % 10; st16(R, WS_O + ((tt * 4 + sg / 10) * 20 + (pi >> 2)) * 1024 + ((pi & 3) * 16 + (lane & 15)) * 16, poison); } }
    }
    f32x16 accq[2]; u32x4 look; unsigned epoch;
    q_proj(x, wq, ws + WS_KVF, tt, hg, smem, accq, look, ws + WS_EPOCH, epoch);
    const unsigned tag = epoch + 1u;
    constexpr int QX = 2 * PSTG;
    {
        const int h = lane >> 5;
#pragma unroll
        for (int c = 0; c < 2; ++c) {
            unsigned pk[6];
#pragma unroll
            for (int i = 0; i < 6; ++i) pk[i] = cvtpk(accq[c][2 * i] * QSCALE, accq[c][2 * i + 1] * QSCALE);
            const unsigned p4 = h ? 0u : pk[4], p5 = h ? 0u : pk[5];
            float ss = dot2bf(pk[0], dot2bf(pk[1], 0.f)) + dot2bf(pk[2], dot2bf(pk[3], 0.f)) + dot2bf(p4, dot2bf(p5, 0.f));
            auto rr = __builtin_amdgcn_permlane32_swap(__float_as_uint(ss), __float_as_uint(ss), false, false);
            const float nrm = __builtin_amdgcn_sqrtf(__uint_as_float(rr[0]) + __uint_as_float(rr[1]));
            const bf16x8 q1 = as8(u32x4{pk[0], pk[1], pk[2], pk[3]});
            const s16x4 q2 = as4(u32x2{h ? __float_as_uint(nrm) : p4, p5});
            *(LAS bf16x8*)(L + QX + (2 * w + c) * 1536 + lane * 16) = q1; *(LAS s16x4*)(L + QX + (2 * w + c) * 1536 + 1024 + lane * 8) = q2;
        }
    }
    LBAR();
    f32x16 o[8];
    float gate;
    {
        bf16x8 q1[8]; u32x2 q2[8];
#pragma unroll
        for (int c = 0; c < 8; ++c) { q1[c] = ldsv(L + QX + c * 1536 + lane * 16); q2[c] = *(LAS u32x2*)(L + QX + c * 1536 + 1024 + lane * 8); }
        LBAR();
        const unsigned offs[2] = {WS_KVF + lane * 16u, WS_KVF + lane * 16u + 8u};
        asm volatile("s_waitcnt vmcnt(0)" : "+{v[156:159]}"(look) :: "memory");
        unsigned pay = max(look[0], look[2]);
        if (!__all(look[1] == tag && look[3] == tag)) pay = poll<2>(ws, offs, tag);
        float kmax = __uint_as_float(wave_max_u32(pay));
        kmax = -1.02f * __builtin_amdgcn_sqrtf(kmax);
        if (lane >= 32) {
#pragma unroll
            for (int c = 0; c < 8; ++c) q2[c].x = cvtpk(__uint_as_float(q2[c].x) * kmax, 0.f);
        }
        PRIO(AP_ATT);
        const unsigned a16 = lane * 16, a8 = lane * 8;
        uint64_t kvp = uni64(ws + WS_KV + (size_t)w * 16 * KVT);
        asm volatile(ATTN_ASM
            : "={v[128:143]}"(o[0]), "={v[144:159]}"(o[1]), "={v[160:175]}"(o[2]), "={v[176:191]}"(o[3]), "={v[192:207]}"(o[4]), "={v[208:223]}"(o[5]), "={v[224:239]}"(o[6]), "={v[240:255]}"(o[7]), "+{s[60:61]}"(kvp), "={v78}"(gate)
            : "{v79}"((unsigned)((lane & 31) * 4)), "{s[64:65]}"(uni64(ws + WS_G + (tt * 2 + cw) * 128)), "{v[80:83]}"(q1[0]), "{v[84:85]}"(q2[0]), "{v[86:89]}"(q1[1]), "{v[90:91]}"(q2[1]), "{v[92:95]}"(q1[2]), "{v[96:97]}"(q2[2]), "{v[98:101]}"(q1[3]), "{v[102:103]}"(q2[3]),
              "{v[104:107]}"(q1[4]), "{v[108:109]}"(q2[4]), "{v[110:113]}"(q1[5]), "{v[114:115]}"(q2[5]), "{v[116:119]}"(q1[6]), "{v[120:121]}"(q2[6]), "{v[122:125]}"(q1[7]), "{v[126:127]}"(q2[7]),
              "{v76}"(a16), "{v77}"(a8), "{v[4:5]}"((u64)tag << 32), "{v6}"(0u), "{s[66:67]}"(uni64(ws + WS_AW + ((tt * 8 + hg) * 4 + w) * 8))
            : ATTN_CLOB);
    }
    lane = fresh_lane();
#pragma unroll
    for (int t = 0; t < 8; ++t) { const int ow = t >> 1;
        if (ow != w) { const int sl = w - (w > ow ? 1 : 0);
#pragma unroll
            for (int g = 0; g < 3; ++g) { f32x4 v = {o[t][4 * g], o[t][4 * g + 1], o[t][4 * g + 2], o[t][4 * g + 3]}; *(LAS f32x4*)(L + (((ow * 3 + sl) * 2 + (t & 1)) * 3 + g) * 1024 + lane * 16) = v; } } }
    LBAR();
    f32x16 mine[2];
    if (w == 0) merge_own<0>(o, mine, L, lane); else if (w == 1) merge_own<1>(o, mine, L, lane); else if (w == 2) merge_own<2>(o, mine, L, lane); else merge_own<3>(o, mine, L, lane);
    {
        bool bad = false;
#pragma unroll
        for (int c = 0; c < 2; ++c) { const float l = mine[c][8]; bad |= (lane >= 32) && !(l >= 5.421011e-20f && l <= 1.0e30f); }
        if (__builtin_expect(__any(bad), 0)) {
            QF q2[2];
#pragma unroll
            for (int c = 0; c < 2; ++c) q_slow(x, wq, tt * 64 + cw * 32 + (lane & 31), hg * 4 + hp * 2 + c, lane >> 5, q2[c]);
            attn_exact(q2, mine, R, lane);
        }
    }
    {
        const int r = lane & 31, h = lane >> 5, tile = tt * 2 + cw;
        const float g = gate;
        const unsigned tbase = WS_O + (tile * 2 + (r >> 4)) * 20 * 1024 + (r & 15) * 16;
        unsigned d4[2][2];
#pragma unroll
        for (int c = 0; c < 2; ++c) {
            auto rr = __builtin_amdgcn_permlane32_swap(__float_as_uint(mine[c][8]), __float_as_uint(mine[c][8]), false, false);
            const float sc = g * __builtin_amdgcn_rcpf(__uint_as_float(rr[1]));
            const int pi = hg * 10 + 2 * (hp * 2 + c) + h;
            st16(R, tbase + (pi >> 2) * 1024 + (pi & 3) * 256, u32x4{cvtpk(mine[c][0] * sc, mine[c][1] * sc), cvtpk(mine[c][2] * sc, mine[c][3] * sc), cvtpk(mine[c][4] * sc, mine[c][5] * sc), cvtpk(mine[c][6] * sc, mine[c][7] * sc)});
            d4[c][0] = cvtpk(mine[c][8] * sc, mine[c][9] * sc); d4[c][1] = cvtpk(mine[c][10] * sc, mine[c][11] * sc);
        }
        if (h == 0) { const int pi = hg * 10 + 8 + hp; st16(R, tbase + (pi >> 2) * 1024 + (pi & 3) * 256, u32x4{d4[0][0], d4[0][1], d4[1][0], d4[1][1]}); }
    }
}

DI f32x2 geglu2(f32x2 g, f32x2 u) {
    const f32x2 z = g * (g * g * -0.1029432397f + -2.302208198f);
    const f32x2 d = f32x2{__builtin_amdgcn_exp2f(z[0]), __builtin_amdgcn_exp2f(z[1])} + 1.0f;
    return (g * u) * f32x2{__builtin_amdgcn_rcpf(d[0]), __builtin_amdgcn_rcpf(d[1])};
}
#define UP3_ASM \
  "s_add_u32 m0, s66, 0\n\t" \
  "s_nop 0\n\t" \
  "global_load_lds_dwordx4 v240, s[60:61]\n\t" \
  "s_add_u32 m0, s66, 1024\n\t" \
  "s_nop 0\n\t" \
  "global_load_lds_dwordx4 v241, s[60:61]\n\t" \
  "s_add_u32 m0, s66, 2048\n\t" \
  "s_nop 0\n\t" \
  "global_load_lds_dwordx4 v242, s[60:61]\n\t" \
  "s_add_u32 m0, s66, 3072\n\t" \
  "s_nop 0\n\t" \
  "global_load_lds_dwordx4 v243, s[60:61]\n\t" \
  "s_add_u32 s60, s60, 128\n\t" \
  "s_addc_u32 s61, s61, 0\n\t" \
  "s_add_u32 m0, s67, 0\n\t" \
  "s_nop 0\n\t" \
  "global_load_lds_dwordx4 v244, s[62:63]\n\t" \
  "s_add_u32 m0, s67, 1024\n\t" \
  "s_nop 0\n\t" \
  "global_load_lds_dwordx4 v245, s[62:63]\n\t" \
  "s_add_u32 m0, s67, 8192\n\t" \
  "s_nop 0\n\t" \
  "global_load_lds_dwordx4 v246, s[64:65]\n\t" \
  "s_add_u32 m0, s67, 9216\n\t" \
  "s_nop 0\n\t" \
  "global_load_lds_dwordx4 v247, s[64:65]\n\t" \
  "s_add_u32 s62, s62, 110592\n\t" \
  "s_addc_u32 s63, s63, 0\n\t" \
  "s_add_u32 s64, s64, 110592\n\t" \
  "s_addc_u32 s65, s65, 0\n\t" \
  "s_add_u32 m0, s67, 16384\n\t" \
  "s_nop 0\n\t" \
  "global_load_lds_dwordx4 v244, s[62:63]\n\t" \
  "s_add_u32 m0, s67, 17408\n\t" \
  "s_nop 0\n\t" \
  "global_load_lds_dwordx4 v245, s[62:63]\n\t" \
  "s_add_u32 m0, s67, 24576\n\t" \
  "s_nop 0\n\t" \
  "global_load_lds_dwordx4 v246, s[64:65]\n\t" \
  "s_add_u32 m0, s67, 25600\n\t" \
  "s_nop 0\n\t" \
  "global_load_lds_dwordx4 v247, s[64:65]\n\t" \
  "s_add_u32 s62, s62, 110592\n\t" \
  "s_addc_u32 s63, s63, 0\n\t" \
  "s_add_u32 s64, s64, 110592\n\t" \
  "s_addc_u32 s65, s65, 0\n\t" \
  "s_add_u32 m0, s66, 16384\n\t" \
  "s_nop 0\n\t" \
  "global_load_lds_dwordx4 v240, s[60:61]\n\t" \
  "s_add_u32 m0, s66, 17408\n\t" \
  "s_nop 0\n\t" \
  "global_load_lds_dwordx4 v241, s[60:61]\n\t" \
  "s_add_u32 m0, s66, 18432\n\t" \
  "s_nop 0\n\t" \
  "global_load_lds_dwordx4 v242, s[60:61]\n\t" \
  "s_add_u32 m0, s66, 19456\n\t" \
  "s_nop 0\n\t" \
  "global_load_lds_dwordx4 v243, s[60:61]\n\t" \
  "s_add_u32 s60, s60, 128\n\t" \
  "s_addc_u32 s61, s61, 0\n\t" \
  "s_add_u32 m0, s67, 32768\n\t" \
  "s_nop 0\n\t" \
  "global_load_lds_dwordx4 v244, s[62:63]\n\t" \
  "s_add_u32 m0, s67, 33792\n\t" \
  "s_nop 0\n\t" \
  "global_load_lds_dwordx4 v245, s[62:63]\n\t" \
  "s_add_u32 m0, s67, 40960\n\t" \
  "s_nop 0\n\t" \
  "global_load_lds_dwordx4 v246, s[64:65]\n\t" \
  "s_add_u32 m0, s67, 41984\n\t" \
  "s_nop 0\n\t" \
  "global_load_lds_dwordx4 v247, s[64:65]\n\t" \
  "s_add_u32 s62, s62, 110592\n\t" \
  "s_addc_u32 s63, s63, 0\n\t" \
  "s_add_u32 s64, s64, 110592\n\t" \
  "s_addc_u32 s65, s65, 0\n\t" \
  "s_waitcnt vmcnt(12)\n\t" \
  "s_barrier\n\t" \
  "ds_read_b128 v[144:147], v248 offset:0\n\t" \
  "ds_read_b128 v[148:151], v248 offset:2048\n\t" \
  "ds_read_b128 v[152:155], v248 offset:4096\n\t" \
  "ds_read_b128 v[156:159], v248 offset:6144\n\t" \
  "ds_read_b64_tr_b16 v[176:177], v250 offset:0\n\t" \
  "ds_read_b64_tr_b16 v[178:179], v250 offset:1024\n\t" \
  "ds_read_b64_tr_b16 v[180:181], v250 offset:8192\n\t" \
  "ds_read_b64_tr_b16 v[182:183], v250 offset:9216\n\t" \
  "ds_read_b64_tr_b16 v[184:185], v251 offset:0\n\t" \
  "ds_read_b64_tr_b16 v[186:187], v251 offset:1024\n\t" \
  "ds_read_b64_tr_b16 v[188:189], v251 offset:8192\n\t" \
  "ds_read_b64_tr_b16 v[190:191], v251 offset:9216\n\t" \
  "ds_read_b64_tr_b16 v[192:193], v252 offset:0\n\t" \
  "ds_read_b64_tr_b16 v[194:195], v252 offset:1024\n\t" \
  "ds_read_b64_tr_b16 v[196:197], v252 offset:8192\n\t" \
  "ds_read_b64_tr_b16 v[198:199], v252 offset:9216\n\t" \
  "ds_read_b64_tr_b16 v[200:201], v253 offset:0\n\t" \
  "ds_read_b64_tr_b16 v[202:203], v253 offset:1024\n\t" \
  "ds_read_b64_tr_b16 v[204:205], v253 offset:8192\n\t" \
  "ds_read_b64_tr_b16 v[206:207], v253 offset:9216\n\t" \
  "s_waitcnt vmcnt(8) lgkmcnt(0)\n\t" \
  "s_barrier\n\t" \
  "v_mfma_f32_16x16x32_bf16 v[16:19], v[176:179], v[144:147], v[16:19]\n\t" \
  "ds_read_b128 v[160:163], v249 offset:0\n\t" \
  "ds_read_b128 v[164:167], v249 offset:2048\n\t" \
  "v_mfma_f32_16x16x32_bf16 v[48:51], v[176:179], v[148:151], v[48:51]\n\t" \
  "ds_read_b128 v[168:171], v249 offset:4096\n\t" \
  "v_mfma_f32_16x16x32_bf16 v[80:83], v[176:179], v[152:155], v[80:83]\n\t" \
  "ds_read_b128 v[172:175], v249 offset:6144\n\t" \
  "v_mfma_f32_16x16x32_bf16 v[112:115], v[176:179], v[156:159], v[112:115]\n\t" \
  "ds_read_b64_tr_b16 v[208:209], v250 offset:16384\n\t" \
  "v_mfma_f32_16x16x32_bf16 v[20:23], v[180:183], v[144:147], v[20:23]\n\t" \
  "ds_read_b64_tr_b16 v[210:211], v250 offset:17408\n\t" \
  "v_mfma_f32_16x16x32_bf16 v[52:55], v[180:183], v[148:151], v[52:55]\n\t" \
  "ds_read_b64_tr_b16 v[212:213], v250 offset:24576\n\t" \
  "v_mfma_f32_16x16x32_bf16 v[84:87], v[180:183], v[152:155], v[84:87]\n\t" \
  "ds_read_b64_tr_b16 v[214:215], v250 offset:25600\n\t" \
  "v_mfma_f32_16x16x32_bf16 v[116:119], v[180:183], v[156:159], v[116:119]\n\t" \
  "ds_read_b64_tr_b16 v[216:217], v251 offset:16384\n\t" \
  "v_mfma_f32_16x16x32_bf16 v[24:27], v[184:187], v[144:147], v[24:27]\n\t" \
  "ds_read_b64_tr_b16 v[218:219], v251 offset:17408\n\t" \
  "v_mfma_f32_16x16x32_bf16 v[56:59], v[184:187], v[148:151], v[56:59]\n\t" \
  "ds_read_b64_tr_b16 v[220:221], v251 offset:24576\n\t" \
  "v_mfma_f32_16x16x32_bf16 v[88:91], v[184:187], v[152:155], v[88:91]\n\t" \
  "ds_read_b64_tr_b16 v[222:223], v251 offset:25600\n\t" \
  "v_mfma_f32_16x16x32_bf16 v[120:123], v[184:187], v[156:159], v[120:123]\n\t" \
  "ds_read_b64_tr_b16 v[224:225], v252 offset:16384\n\t" \
  "v_mfma_f32_16x16x32_bf16 v[28:31], v[188:191], v[144:147], v[28:31]\n\t" \
  "ds_read_b64_tr_b16 v[226:227], v252 offset:17408\n\t" \
  "v_mfma_f32_16x16x32_bf16 v[60:63], v[188:191], v[148:151], v[60:63]\n\t" \
  "ds_read_b64_tr_b16 v[228:229], v252 offset:24576\n\t" \
  "v_mfma_f32_16x16x32_bf16 v[92:95], v[188:191], v[152:155], v[92:95]\n\t" \
  "ds_read_b64_tr_b16 v[230:231], v252 offset:25600\n\t" \
  "v_mfma_f32_16x16x32_bf16 v[124:127], v[188:191], v[156:159], v[124:127]\n\t" \
  "ds_read_b64_tr_b16 v[232:233], v253 offset:16384\n\t" \
  "v_mfma_f32_16x16x32_bf16 v[32:35], v[192:195], v[144:147], v[32:35]\n\t" \
  "ds_read_b64_tr_b16 v[234:235], v253 offset:17408\n\t" \
  "v_mfma_f32_16x16x32_bf16 v[64:67], v[192:195], v[148:151], v[64:67]\n\t" \
  "ds_read_b64_tr_b16 v[236:237], v253 offset:24576\n\t" \
  "v_mfma_f32_16x16x32_bf16 v[96:99], v[192:195], v[152:155], v[96:99]\n\t" \
  "ds_read_b64_tr_b16 v[238:239], v253 offset:25600\n\t" \
  "v_mfma_f32_16x16x32_bf16 v[128:131], v[192:195], v[156:159], v[128:131]\n\t" \
  "s_add_u32 m0, s67, 0\n\t" \
  "s_nop 0\n\t" \
  "global_load_lds_dwordx4 v244, s[62:63]\n\t" \
  "v_mfma_f32_16x16x32_bf16 v[36:39], v[196:199], v[144:147], v[36:39]\n\t" \
  "s_add_u32 m0, s67, 1024\n\t" \
  "s_nop 0\n\t" \
  "global_load_lds_dwordx4 v245, s[62:63]\n\t" \
  "v_mfma_f32_16x16x32_bf16 v[68:71], v[196:199], v[148:151], v[68:71]\n\t" \
  "s_add_u32 m0, s67, 8192\n\t" \
  "s_nop 0\n\t" \
  "global_load_lds_dwordx4 v246, s[64:65]\n\t" \
  "v_mfma_f32_16x16x32_bf16 v[100:103], v[196:199], v[152:155], v[100:103]\n\t" \
  "s_add_u32 m0, s67, 9216\n\t" \
  "s_nop 0\n\t" \
  "global_load_lds_dwordx4 v247, s[64:65]\n\t" \
  "v_mfma_f32_16x16x32_bf16 v[132:135], v[196:199], v[156:159], v[132:135]\n\t" \
  "s_add_u32 s62, s62, 110592\n\t" \
  "s_addc_u32 s63, s63, 0\n\t" \
  "s_add_u32 s64, s64, 110592\n\t" \
  "s_addc_u32 s65, s65, 0\n\t" \
  "v_mfma_f32_16x16x32_bf16 v[40:43], v[200:203], v[144:147], v[40:43]\n\t" \
  "v_mfma_f32_16x16x32_bf16 v[72:75], v[200:203], v[148:151], v[72:75]\n\t" \
  "v_mfma_f32_16x16x32_bf16 v[104:107], v[200:203], v[152:155], v[104:107]\n\t" \
  "v_mfma_f32_16x16x32_bf16 v[136:139], v[200:203], v[156:159], v[136:139]\n\t" \
  "v_mfma_f32_16x16x32_bf16 v[44:47], v[204:207], v[144:147], v[44:47]\n\t" \
  "v_mfma_f32_16x16x32_bf16 v[76:79], v[204:207], v[148:151], v[76:79]\n\t" \
  "v_mfma_f32_16x16x32_bf16 v[108:111], v[204:207], v[152:155], v[108:111]\n\t" \
  "v_mfma_f32_16x16x32_bf16 v[140:143], v[204:207], v[156:159], v[140:143]\n\t" \
  "s_waitcnt vmcnt(4) lgkmcnt(0)\n\t" \
  "s_barrier\n\t" \
  "v_mfma_f32_16x16x32_bf16 v[16:19], v[208:211], v[160:163], v[16:19]\n\t" \
  "ds_read_b128 v[144:147], v248 offset:16384\n\t" \
  "ds_read_b128 v[148:151], v248 offset:18432\n\t" \
  "v_mfma_f32_16x16x32_bf16 v[48:51], v[208:211], v[164:167], v[48:51]\n\t" \
  "ds_read_b128 v[152:155], v248 offset:20480\n\t" \
  "v_mfma_f32_16x16x32_bf16 v[80:83], v[208:211], v[168:171], v[80:83]\n\t" \
  "ds_read_b128 v[156:159], v248 offset:22528\n\t" \
  "v_mfma_f32_16x16x32_bf16 v[112:115], v[208:211], v[172:175], v[112:115]\n\t" \
  "ds_read_b64_tr_b16 v[176:177], v250 offset:32768\n\t" \
  "v_mfma_f32_16x16x32_bf16 v[20:23], v[212:215], v[160:163], v[20:23]\n\t" \
  "ds_read_b64_tr_b16 v[178:179], v250 offset:33792\n\t" \
  "ds_read_b64_tr_b16 v[180:181], v250 offset:40960\n\t" \
  "v_mfma_f32_16x16x32_bf16 v[52:55], v[212:215], v[164:167], v[52:55]\n\t" \
  "ds_read_b64_tr_b16 v[182:183], v250 offset:41984\n\t" \
  "v_mfma_f32_16x16x32_bf16 v[84:87], v[212:215], v[168:171], v[84:87]\n\t" \
  "ds_read_b64_tr_b16 v[184:185], v251 offset:32768\n\t" \
  "v_mfma_f32_16x16x32_bf16 v[116:119], v[212:215], v[172:175], v[116:119]\n\t" \
  "ds_read_b64_tr_b16 v[186:187], v251 offset:33792\n\t" \
  "v_mfma_f32_16x16x32_bf16 v[24:27], v[216:219], v[160:163], v[24:27]\n\t" \
  "ds_read_b64_tr_b16 v[188:189], v251 offset:40960\n\t" \
  "ds_read_b64_tr_b16 v[190:191], v251 offset:41984\n\t" \
  "v_mfma_f32_16x16x32_bf16 v[56:59], v[216:219], v[164:167], v[56:59]\n\t" \
  "ds_read_b64_tr_b16 v[192:193], v252 offset:32768\n\t" \
  "v_mfma_f32_16x16x32_bf16 v[88:91], v[216:219], v[168:171], v[88:91]\n\t" \
  "ds_read_b64_tr_b16 v[194:195], v252 offset:33792\n\t" \
  "v_mfma_f32_16x16x32_bf16 v[120:123], v[216:219], v[172:175], v[120:123]\n\t" \
  "ds_read_b64_tr_b16 v[196:197], v252 offset:40960\n\t" \
  "v_mfma_f32_16x16x32_bf16 v[28:31], v[220:223], v[160:163], v[28:31]\n\t" \
  "ds_read_b64_tr_b16 v[198:199], v252 offset:41984\n\t" \
  "ds_read_b64_tr_b16 v[200:201], v253 offset:32768\n\t" \
  "v_mfma_f32_16x16x32_bf16 v[60:63], v[220:223], v[164:167], v[60:63]\n\t" \
  "ds_read_b64_tr_b16 v[202:203], v253 offset:33792\n\t" \
  "v_mfma_f32_16x16x32_bf16 v[92:95], v[220:223], v[168:171], v[92:95]\n\t" \
  "ds_read_b64_tr_b16 v[204:205], v253 offset:40960\n\t" \
  "v_mfma_f32_16x16x32_bf16 v[124:127], v[220:223], v[172:175], v[124:127]\n\t" \
  "ds_read_b64_tr_b16 v[206:207], v253 offset:41984\n\t" \
  "v_mfma_f32_16x16x32_bf16 v[32:35], v[224:227], v[160:163], v[32:35]\n\t" \
  "s_add_u32 m0, s66, 0\n\t" \
  "s_nop 0\n\t" \
  "global_load_lds_dwordx4 v240, s[60:61]\n\t" \
  "s_add_u32 m0, s66, 1024\n\t" \
  "s_nop 0\n\t" \
  "global_load_lds_dwordx4 v241, s[60:61]\n\t" \
  "v_mfma_f32_16x16x32_bf16 v[64:67], v[224:227], v[164:167], v[64:67]\n\t" \
  "s_add_u32 m0, s66, 2048\n\t" \
  "s_nop 0\n\t" \
  "global_load_lds_dwordx4 v242, s[60:61]\n\t" \
  "v_mfma_f32_16x16x32_bf16 v[96:99], v[224:227], v[168:171], v[96:99]\n\t" \
  "s_add_u32 m0, s66, 3072\n\t" \
  "s_nop 0\n\t" \
  "global_load_lds_dwordx4 v243, s[60:61]\n\t" \
  "v_mfma_f32_16x16x32_bf16 v[128:131], v[224:227], v[172:175], v[128:131]\n\t" \
  "s_add_u32 s60, s60, 128\n\t" \
  "s_addc_u32 s61, s61, 0\n\t" \
  "v_mfma_f32_16x16x32_bf16 v[36:39], v[228:231], v[160:163], v[36:39]\n\t" \
  "s_add_u32 m0, s67, 16384\n\t" \
  "s_nop 0\n\t" \
  "global_load_lds_dwordx4 v244, s[62:63]\n\t" \
  "s_add_u32 m0, s67, 17408\n\t" \
  "s_nop 0\n\t" \
  "global_load_lds_dwordx4 v245, s[62:63]\n\t" \
  "v_mfma_f32_16x16x32_bf16 v[68:71], v[228:231], v[164:167], v[68:71]\n\t" \
  "s_add_u32 m0, s67, 24576\n\t" \
  "s_nop 0\n\t" \
  "global_load_lds_dwordx4 v246, s[64:65]\n\t" \
  "v_mfma_f32_16x16x32_bf16 v[100:103], v[228:231], v[168:171], v[100:103]\n\t" \
  "s_add_u32 m0, s67, 25600\n\t" \
  "s_nop 0\n\t" \
  "global_load_lds_dwordx4 v247, s[64:65]\n\t" \
  "v_mfma_f32_16x16x32_bf16 v[132:135], v[228:231], v[172:175], v[132:135]\n\t" \
  "s_add_u32 s62, s62, 110592\n\t" \
  "s_addc_u32 s63, s63, 0\n\t" \
  "s_add_u32 s64, s64, 110592\n\t" \
  "s_addc_u32 s65, s65, 0\n\t" \
  "v_mfma_f32_16x16x32_bf16 v[40:43], v[232:235], v[160:163], v[40:43]\n\t" \
  "v_mfma_f32_16x16x32_bf16 v[72:75], v[232:235], v[164:167], v[72:75]\n\t" \
  "v_mfma_f32_16x16x32_bf16 v[104:107], v[232:235], v[168:171], v[104:107]\n\t" \
  "v_mfma_f32_16x16x32_bf16 v[136:139], v[232:235], v[172:175], v[136:139]\n\t" \
  "v_mfma_f32_16x16x32_bf16 v[44:47], v[236:239], v[160:163], v[44:47]\n\t" \
  "v_mfma_f32_16x16x32_bf16 v[76:79], v[236:239], v[164:167], v[76:79]\n\t" \
  "v_mfma_f32_16x16x32_bf16 v[108:111], v[236:239], v[168:171], v[108:111]\n\t" \
  "v_mfma_f32_16x16x32_bf16 v[140:143], v[236:239], v[172:175], v[140:143]\n\t" \
  "s_waitcnt vmcnt(8) lgkmcnt(0)\n\t" \
  "s_barrier\n\t" \
  "v_mfma_f32_16x16x32_bf16 v[16:19], v[176:179], v[144:147], v[16:19]\n\t" \
  "ds_read_b128 v[160:163], v249 offset:16384\n\t" \
  "ds_read_b128 v[164:167], v249 offset:18432\n\t" \
  "v_mfma_f32_16x16x32_bf16 v[48:51], v[176:179], v[148:151], v[48:51]\n\t" \
  "ds_read_b128 v[168:171], v249 offset:20480\n\t" \
  "v_mfma_f32_16x16x32_bf16 v[80:83], v[176:179], v[152:155], v[80:83]\n\t" \
  "ds_read_b128 v[172:175], v249 offset:22528\n\t" \
  "v_mfma_f32_16x16x32_bf16 v[112:115], v[176:179], v[156:159], v[112:115]\n\t" \
  "ds_read_b64_tr_b16 v[208:209], v250 offset:0\n\t" \
  "v_mfma_f32_16x16x32_bf16 v[20:23], v[180:183], v[144:147], v[20:23]\n\t" \
  "ds_read_b64_tr_b16 v[210:211], v250 offset:1024\n\t" \
  "v_mfma_f32_16x16x32_bf16 v[52:55], v[180:183], v[148:151], v[52:55]\n\t" \
  "ds_read_b64_tr_b16 v[212:213], v250 offset:8192\n\t" \
  "v_mfma_f32_16x16x32_bf16 v[84:87], v[180:183], v[152:155], v[84:87]\n\t" \
  "ds_read_b64_tr_b16 v[214:215], v250 offset:9216\n\t" \
  "v_mfma_f32_16x16x32_bf16 v[116:119], v[180:183], v[156:159], v[116:119]\n\t" \
  "ds_read_b64_tr_b16 v[216:217], v251 offset:0\n\t" \
  "v_mfma_f32_16x16x32_bf16 v[24:27], v[184:187], v[144:147], v[24:27]\n\t" \
  "ds_read_b64_tr_b16 v[218:219], v251 offset:1024\n\t" \
  "v_mfma_f32_16x16x32_bf16 v[56:59], v[184:187], v[148:151], v[56:59]\n\t" \
  "ds_read_b64_tr_b16 v[220:221], v251 offset:8192\n\t" \
  "v_mfma_f32_16x16x32_bf16 v[88:91], v[184:187], v[152:155], v[88:91]\n\t" \
  "ds_read_b64_tr_b16 v[222:223], v251 offset:9216\n\t" \
  "v_mfma_f32_16x16x32_bf16 v[120:123], v[184:187], v[156:159], v[120:123]\n\t" \
  "ds_read_b64_tr_b16 v[224:225], v252 offset:0\n\t" \
  "v_mfma_f32_16x16x32_bf16 v[28:31], v[188:191], v[144:147], v[28:31]\n\t" \
  "ds_read_b64_tr_b16 v[226:227], v252 offset:1024\n\t" \
  "v_mfma_f32_16x16x32_bf16 v[60:63], v[188:191], v[148:151], v[60:63]\n\t" \
  "ds_read_b64_tr_b16 v[228:229], v252 offset:8192\n\t" \
  "v_mfma_f32_16x16x32_bf16 v[92:95], v[188:191], v[152:155], v[92:95]\n\t" \
  "ds_read_b64_tr_b16 v[230:231], v252 offset:9216\n\t" \
  "v_mfma_f32_16x16x32_bf16 v[124:127], v[188:191], v[156:159], v[124:127]\n\t" \
  "ds_read_b64_tr_b16 v[232:233], v253 offset:0\n\t" \
  "v_mfma_f32_16x16x32_bf16 v[32:35], v[192:195], v[144:147], v[32:35]\n\t" \
  "ds_read_b64_tr_b16 v[234:235], v253 offset:1024\n\t" \
  "v_mfma_f32_16x16x32_bf16 v[64:67], v[192:195], v[148:151], v[64:67]\n\t" \
  "ds_read_b64_tr_b16 v[236:237], v253 offset:8192\n\t" \
  "v_mfma_f32_16x16x32_bf16 v[96:99], v[192:195], v[152:155], v[96:99]\n\t" \
  "ds_read_b64_tr_b16 v[238:239], v253 offset:9216\n\t" \
  "v_mfma_f32_16x16x32_bf16 v[128:131], v[192:195], v[156:159], v[128:131]\n\t" \
  "s_add_u32 m0, s67, 32768\n\t" \
  "s_nop 0\n\t" \
  "global_load_lds_dwordx4 v244, s[62:63]\n\t" \
  "v_mfma_f32_16x16x32_bf16 v[36:39], v[196:199], v[144:147], v[36:39]\n\t" \
  "s_add_u32 m0, s67, 33792\n\t" \
  "s_nop 0\n\t" \
  "global_load_lds_dwordx4 v245, s[62:63]\n\t" \
  "v_mfma_f32_16x16x32_bf16 v[68:71], v[196:199], v[148:151], v[68:71]\n\t" \
  "s_add_u32 m0, s67, 40960\n\t" \
  "s_nop 0\n\t" \
  "global_load_lds_dwordx4 v246, s[64:65]\n\t" \
  "v_mfma_f32_16x16x32_bf16 v[100:103], v[196:199], v[152:155], v[100:103]\n\t" \
  "s_add_u32 m0, s67, 41984\n\t" \
  "s_nop 0\n\t" \
  "global_load_lds_dwordx4 v247, s[64:65]\n\t" \
  "v_mfma_f32_16x16x32_bf16 v[132:135], v[196:199], v[156:159], v[132:135]\n\t" \
  "s_add_u32 s62, s62, 110592\n\t" \
  "s_addc_u32 s63, s63, 0\n\t" \
  "s_add_u32 s64, s64, 110592\n\t" \
  "s_addc_u32 s65, s65, 0\n\t" \
  "v_mfma_f32_16x16x32_bf16 v[40:43], v[200:203], v[144:147], v[40:43]\n\t" \
  "v_mfma_f32_16x16x32_bf16 v[72:75], v[200:203], v[148:151], v[72:75]\n\t" \
  "v_mfma_f32_16x16x32_bf16 v[104:107], v[200:203], v[152:155], v[104:107]\n\t" \
  "v_mfma_f32_16x16x32_bf16 v[136:139], v[200:203], v[156:159], v[136:139]\n\t" \
  "v_mfma_f32_16x16x32_bf16 v[44:47], v[204:207], v[144:147], v[44:47]\n\t" \
  "v_mfma_f32_16x16x32_bf16 v[76:79], v[204:207], v[148:151], v[76:79]\n\t" \
  "v_mfma_f32_16x16x32_bf16 v[108:111], v[204:207], v[152:155], v[108:111]\n\t" \
  "v_mfma_f32_16x16x32_bf16 v[140:143], v[204:207], v[156:159], v[140:143]\n\t" \
  "s_waitcnt vmcnt(4) lgkmcnt(0)\n\t" \
  "s_barrier\n\t" \
  "v_mfma_f32_16x16x32_bf16 v[16:19], v[208:211], v[160:163], v[16:19]\n\t" \
  "ds_read_b128 v[144:147], v248 offset:0\n\t" \
  "ds_read_b128 v[148:151], v248 offset:2048\n\t" \
  "v_mfma_f32_16x16x32_bf16 v[48:51], v[208:211], v[164:167], v[48:51]\n\t" \
  "ds_read_b128 v[152:155], v248 offset:4096\n\t" \
  "v_mfma_f32_16x16x32_bf16 v[80:83], v[208:211], v[168:171], v[80:83]\n\t" \
  "ds_read_b128 v[156:159], v248 offset:6144\n\t" \
  "v_mfma_f32_16x16x32_bf16 v[112:115], v[208:211], v[172:175], v[112:115]\n\t" \
  "ds_read_b64_tr_b16 v[176:177], v250 offset:16384\n\t" \
  "v_mfma_f32_16x16x32_bf16 v[20:23], v[212:215], v[160:163], v[20:23]\n\t" \
  "ds_read_b64_tr_b16 v[178:179], v250 offset:17408\n\t" \
  "ds_read_b64_tr_b16 v[180:181], v250 offset:24576\n\t" \
  "v_mfma_f32_16x16x32_bf16 v[52:55], v[212:215], v[164:167], v[52:55]\n\t" \
  "ds_read_b64_tr_b16 v[182:183], v250 offset:25600\n\t" \
  "v_mfma_f32_16x16x32_bf16 v[84:87], v[212:215], v[168:171], v[84:87]\n\t" \
  "ds_read_b64_tr_b16 v[184:185], v251 offset:16384\n\t" \
  "v_mfma_f32_16x16x32_bf16 v[116:119], v[212:215], v[172:175], v[116:119]\n\t" \
  "ds_read_b64_tr_b16 v[186:187], v251 offset:17408\n\t" \
  "v_mfma_f32_16x16x32_bf16 v[24:27], v[216:219], v[160:163], v[24:27]\n\t" \
  "ds_read_b64_tr_b16 v[188:189], v251 offset:24576\n\t" \
  "ds_read_b64_tr_b16 v[190:191], v251 offset:25600\n\t" \
  "v_mfma_f32_16x16x32_bf16 v[56:59], v[216:219], v[164:167], v[56:59]\n\t" \
  "ds_read_b64_tr_b16 v[192:193], v252 offset:16384\n\t" \
  "v_mfma_f32_16x16x32_bf16 v[88:91], v[216:219], v[168:171], v[88:91]\n\t" \
  "ds_read_b64_tr_b16 v[194:195], v252 offset:17408\n\t" \
  "v_mfma_f32_16x16x32_bf16 v[120:123], v[216:219], v[172:175], v[120:123]\n\t" \
  "ds_read_b64_tr_b16 v[196:197], v252 offset:24576\n\t" \
  "v_mfma_f32_16x16x32_bf16 v[28:31], v[220:223], v[160:163], v[28:31]\n\t" \
  "ds_read_b64_tr_b16 v[198:199], v252 offset:25600\n\t" \
  "ds_read_b64_tr_b16 v[200:201], v253 offset:16384\n\t" \
  "v_mfma_f32_16x16x32_bf16 v[60:63], v[220:223], v[164:167], v[60:63]\n\t" \
  "ds_read_b64_tr_b16 v[202:203], v253 offset:17408\n\t" \
  "v_mfma_f32_16x16x32_bf16 v[92:95], v[220:223], v[168:171], v[92:95]\n\t" \
  "ds_read_b64_tr_b16 v[204:205], v253 offset:24576\n\t" \
  "v_mfma_f32_16x16x32_bf16 v[124:127], v[220:223], v[172:175], v[124:127]\n\t" \
  "ds_read_b64_tr_b16 v[206:207], v253 offset:25600\n\t" \
  "v_mfma_f32_16x16x32_bf16 v[32:35], v[224:227], v[160:163], v[32:35]\n\t" \
  "s_add_u32 m0, s66, 16384\n\t" \
  "s_nop 0\n\t" \
  "global_load_lds_dwordx4 v240, s[60:61]\n\t" \
  "s_add_u32 m0, s66, 17408\n\t" \
  "s_nop 0\n\t" \
  "global_load_lds_dwordx4 v241, s[60:61]\n\t" \
  "v_mfma_f32_16x16x32_bf16 v[64:67], v[224:227], v[164:167], v[64:67]\n\t" \
  "s_add_u32 m0, s66, 18432\n\t" \
  "s_nop 0\n\t" \
  "global_load_lds_dwordx4 v242, s[60:61]\n\t" \
  "v_mfma_f32_16x16x32_bf16 v[96:99], v[224:227], v[168:171], v[96:99]\n\t" \
  "s_add_u32 m0, s66, 19456\n\t" \
  "s_nop 0\n\t" \
  "global_load_lds_dwordx4 v243, s[60:61]\n\t" \
  "v_mfma_f32_16x16x32_bf16 v[128:131], v[224:227], v[172:175], v[128:131]\n\t" \
  "s_add_u32 s60, s60, 128\n\t" \
  "s_addc_u32 s61, s61, 0\n\t" \
  "v_mfma_f32_16x16x32_bf16 v[36:39], v[228:231], v[160:163], v[36:39]\n\t" \
  "s_add_u32 m0, s67, 0\n\t" \
  "s_nop 0\n\t" \
  "global_load_lds_dwordx4 v244, s[62:63]\n\t" \
  "s_add_u32 m0, s67, 1024\n\t" \
  "s_nop 0\n\t" \
  "global_load_lds_dwordx4 v245, s[62:63]\n\t" \
  "v_mfma_f32_16x16x32_bf16 v[68:71], v[228:231], v[164:167], v[68:71]\n\t" \
  "s_add_u32 m0, s67, 8192\n\t" \
  "s_nop 0\n\t" \
  "global_load_lds_dwordx4 v246, s[64:65]\n\t" \
  "v_mfma_f32_16x16x32_bf16 v[100:103], v[228:231], v[168:171], v[100:103]\n\t" \
  "s_add_u32 m0, s67, 9216\n\t" \
  "s_nop 0\n\t" \
  "global_load_lds_dwordx4 v247, s[64:65]\n\t" \
  "v_mfma_f32_16x16x32_bf16 v[132:135], v[228:231], v[172:175], v[132:135]\n\t" \
  "s_add_u32 s62, s62, 110592\n\t" \
  "s_addc_u32 s63, s63, 0\n\t" \
  "s_add_u32 s64, s64, 110592\n\t" \
  "s_addc_u32 s65, s65, 0\n\t" \
  "v_mfma_f32_16x16x32_bf16 v[40:43], v[232:235], v[160:163], v[40:43]\n\t" \
  "v_mfma_f32_16x16x32_bf16 v[72:75], v[232:235], v[164:167], v[72:75]\n\t" \
  "v_mfma_f32_16x16x32_bf16 v[104:107], v[232:235], v[168:171], v[104:107]\n\t" \
  "v_mfma_f32_16x16x32_bf16 v[136:139], v[232:235], v[172:175], v[136:139]\n\t" \
  "v_mfma_f32_16x16x32_bf16 v[44:47], v[236:239], v[160:163], v[44:47]\n\t" \
  "v_mfma_f32_16x16x32_bf16 v[76:79], v[236:239], v[164:167], v[76:79]\n\t" \
  "v_mfma_f32_16x16x32_bf16 v[108:111], v[236:239], v[168:171], v[108:111]\n\t" \
  "v_mfma_f32_16x16x32_bf16 v[140:143], v[236:239], v[172:175], v[140:143]\n\t" \
  "s_waitcnt vmcnt(8) lgkmcnt(0)\n\t" \
  "s_barrier\n\t" \
  "v_mfma_f32_16x16x32_bf16 v[16:19], v[176:179], v[144:147], v[16:19]\n\t" \
  "ds_read_b128 v[160:163], v249 offset:0\n\t" \
  "ds_read_b128 v[164:167], v249 offset:2048\n\t" \
  "v_mfma_f32_16x16x32_bf16 v[48:51], v[176:179], v[148:151], v[48:51]\n\t" \
  "ds_read_b128 v[168:171], v249 offset:4096\n\t" \
  "v_mfma_f32_16x16x32_bf16 v[80:83], v[176:179], v[152:155], v[80:83]\n\t" \
  "ds_read_b128 v[172:175], v249 offset:6144\n\t" \
  "v_mfma_f32_16x16x32_bf16 v[112:115], v[176:179], v[156:159], v[112:115]\n\t" \
  "ds_read_b64_tr_b16 v[208:209], v250 offset:32768\n\t" \
  "v_mfma_f32_16x16x32_bf16 v[20:23], v[180:183], v[144:147], v[20:23]\n\t" \
  "ds_read_b64_tr_b16 v[210:211], v250 offset:33792\n\t" \
  "v_mfma_f32_16x16x32_bf16 v[52:55], v[180:183], v[148:151], v[52:55]\n\t" \
  "ds_read_b64_tr_b16 v[212:213], v250 offset:40960\n\t" \
  "v_mfma_f32_16x16x32_bf16 v[84:87], v[180:183], v[152:155], v[84:87]\n\t" \
  "ds_read_b64_tr_b16 v[214:215], v250 offset:41984\n\t" \
  "v_mfma_f32_16x16x32_bf16 v[116:119], v[180:183], v[156:159], v[116:119]\n\t" \
  "ds_read_b64_tr_b16 v[216:217], v251 offset:32768\n\t" \
  "v_mfma_f32_16x16x32_bf16 v[24:27], v[184:187], v[144:147], v[24:27]\n\t" \
  "ds_read_b64_tr_b16 v[218:219], v251 offset:33792\n\t" \
  "v_mfma_f32_16x16x32_bf16 v[56:59], v[184:187], v[148:151], v[56:59]\n\t" \
  "ds_read_b64_tr_b16 v[220:221], v251 offset:40960\n\t" \
  "v_mfma_f32_16x16x32_bf16 v[88:91], v[184:187], v[152:155], v[88:91]\n\t" \
  "ds_read_b64_tr_b16 v[222:223], v251 offset:41984\n\t" \
  "v_mfma_f32_16x16x32_bf16 v[120:123], v[184:187], v[156:159], v[120:123]\n\t" \
  "ds_read_b64_tr_b16 v[224:225], v252 offset:32768\n\t" \
  "v_mfma_f32_16x16x32_bf16 v[28:31], v[188:191], v[144:147], v[28:31]\n\t" \
  "ds_read_b64_tr_b16 v[226:227], v252 offset:33792\n\t" \
  "v_mfma_f32_16x16x32_bf16 v[60:63], v[188:191], v[148:151], v[60:63]\n\t" \
  "ds_read_b64_tr_b16 v[228:229], v252 offset:40960\n\t" \
  "v_mfma_f32_16x16x32_bf16 v[92:95], v[188:191], v[152:155], v[92:95]\n\t" \
  "ds_read_b64_tr_b16 v[230:231], v252 offset:41984\n\t" \
  "v_mfma_f32_16x16x32_bf16 v[124:127], v[188:191], v[156:159], v[124:127]\n\t" \
  "ds_read_b64_tr_b16 v[232:233], v253 offset:32768\n\t" \
  "v_mfma_f32_16x16x32_bf16 v[32:35], v[192:195], v[144:147], v[32:35]\n\t" \
  "ds_read_b64_tr_b16 v[234:235], v253 offset:33792\n\t" \
  "v_mfma_f32_16x16x32_bf16 v[64:67], v[192:195], v[148:151], v[64:67]\n\t" \
  "ds_read_b64_tr_b16 v[236:237], v253 offset:40960\n\t" \
  "v_mfma_f32_16x16x32_bf16 v[96:99], v[192:195], v[152:155], v[96:99]\n\t" \
  "ds_read_b64_tr_b16 v[238:239], v253 offset:41984\n\t" \
  "v_mfma_f32_16x16x32_bf16 v[128:131], v[192:195], v[156:159], v[128:131]\n\t" \
  "s_add_u32 m0, s67, 16384\n\t" \
  "s_nop 0\n\t" \
  "global_load_lds_dwordx4 v244, s[62:63]\n\t" \
  "v_mfma_f32_16x16x32_bf16 v[36:39], v[196:199], v[144:147], v[36:39]\n\t" \
  "s_add_u32 m0, s67, 17408\n\t" \
  "s_nop 0\n\t" \
  "global_load_lds_dwordx4 v245, s[62:63]\n\t" \
  "v_mfma_f32_16x16x32_bf16 v[68:71], v[196:199], v[148:151], v[68:71]\n\t" \
  "s_add_u32 m0, s67, 24576\n\t" \
  "s_nop 0\n\t" \
  "global_load_lds_dwordx4 v246, s[64:65]\n\t" \
  "v_mfma_f32_16x16x32_bf16 v[100:103], v[196:199], v[152:155], v[100:103]\n\t" \
  "s_add_u32 m0, s67, 25600\n\t" \
  "s_nop 0\n\t" \
  "global_load_lds_dwordx4 v247, s[64:65]\n\t" \
  "v_mfma_f32_16x16x32_bf16 v[132:135], v[196:199], v[156:159], v[132:135]\n\t" \
  "s_add_u32 s62, s62, 110592\n\t" \
  "s_addc_u32 s63, s63, 0\n\t" \
  "s_add_u32 s64, s64, 110592\n\t" \
  "s_addc_u32 s65, s65, 0\n\t" \
  "v_mfma_f32_16x16x32_bf16 v[40:43], v[200:203], v[144:147], v[40:43]\n\t" \
  "v_mfma_f32_16x16x32_bf16 v[72:75], v[200:203], v[148:151], v[72:75]\n\t" \
  "v_mfma_f32_16x16x32_bf16 v[104:107], v[200:203], v[152:155], v[104:107]\n\t" \
  "v_mfma_f32_16x16x32_bf16 v[136:139], v[200:203], v[156:159], v[136:139]\n\t" \
  "v_mfma_f32_16x16x32_bf16 v[44:47], v[204:207], v[144:147], v[44:47]\n\t" \
  "v_mfma_f32_16x16x32_bf16 v[76:79], v[204:207], v[148:151], v[76:79]\n\t" \
  "v_mfma_f32_16x16x32_bf16 v[108:111], v[204:207], v[152:155], v[108:111]\n\t" \
  "v_mfma_f32_16x16x32_bf16 v[140:143], v[204:207], v[156:159], v[140:143]\n\t" \
  "s_waitcnt vmcnt(4) lgkmcnt(0)\n\t" \
  "s_barrier\n\t" \
  "v_mfma_f32_16x16x32_bf16 v[16:19], v[208:211], v[160:163], v[16:19]\n\t" \
  "ds_read_b128 v[144:147], v248 offset:16384\n\t" \
  "ds_read_b128 v[148:151], v248 offset:18432\n\t" \
  "v_mfma_f32_16x16x32_bf16 v[48:51], v[208:211], v[164:167], v[48:51]\n\t" \
  "ds_read_b128 v[152:155], v248 offset:20480\n\t" \
  "v_mfma_f32_16x16x32_bf16 v[80:83], v[208:211], v[168:171], v[80:83]\n\t" \
  "ds_read_b128 v[156:159], v248 offset:22528\n\t" \
  "v_mfma_f32_16x16x32_bf16 v[112:115], v[208:211], v[172:175], v[112:115]\n\t" \
  "ds_read_b64_tr_b16 v[176:177], v250 offset:0\n\t" \
  "v_mfma_f32_16x16x32_bf16 v[20:23], v[212:215], v[160:163], v[20:23]\n\t" \
  "ds_read_b64_tr_b16 v[178:179], v250 offset:1024\n\t" \
  "ds_read_b64_tr_b16 v[180:181], v250 offset:8192\n\t" \
  "v_mfma_f32_16x16x32_bf16 v[52:55], v[212:215], v[164:167], v[52:55]\n\t" \
  "ds_read_b64_tr_b16 v[182:183], v250 offset:9216\n\t" \
  "v_mfma_f32_16x16x32_bf16 v[84:87], v[212:215], v[168:171], v[84:87]\n\t" \
  "ds_read_b64_tr_b16 v[184:185], v251 offset:0\n\t" \
  "v_mfma_f32_16x16x32_bf16 v[116:119], v[212:215], v[172:175], v[116:119]\n\t" \
  "ds_read_b64_tr_b16 v[186:187], v251 offset:1024\n\t" \
  "v_mfma_f32_16x16x32_bf16 v[24:27], v[216:219], v[160:163], v[24:27]\n\t" \
  "ds_read_b64_tr_b16 v[188:189], v251 offset:8192\n\t" \
  "ds_read_b64_tr_b16 v[190:191], v251 offset:9216\n\t" \
  "v_mfma_f32_16x16x32_bf16 v[56:59], v[216:219], v[164:167], v[56:59]\n\t" \
  "ds_read_b64_tr_b16 v[192:193], v252 offset:0\n\t" \
  "v_mfma_f32_16x16x32_bf16 v[88:91], v[216:219], v[168:171], v[88:91]\n\t" \
  "ds_read_b64_tr_b16 v[194:195], v252 offset:1024\n\t" \
  "v_mfma_f32_16x16x32_bf16 v[120:123], v[216:219], v[172:175], v[120:123]\n\t" \
  "ds_read_b64_tr_b16 v[196:197], v252 offset:8192\n\t" \
  "v_mfma_f32_16x16x32_bf16 v[28:31], v[220:223], v[160:163], v[28:31]\n\t" \
  "ds_read_b64_tr_b16 v[198:199], v252 offset:9216\n\t" \
  "ds_read_b64_tr_b16 v[200:201], v253 offset:0\n\t" \
  "v_mfma_f32_16x16x32_bf16 v[60:63], v[220:223], v[164:167], v[60:63]\n\t" \
  "ds_read_b64_tr_b16 v[202:203], v253 offset:1024\n\t" \
  "v_mfma_f32_16x16x32_bf16 v[92:95], v[220:223], v[168:171], v[92:95]\n\t" \
  "ds_read_b64_tr_b16 v[204:205], v253 offset:8192\n\t" \
  "v_mfma_f32_16x16x32_bf16 v[124:127], v[220:223], v[172:175], v[124:127]\n\t" \
  "ds_read_b64_tr_b16 v[206:207], v253 offset:9216\n\t" \
  "v_mfma_f32_16x16x32_bf16 v[32:35], v[224:227], v[160:163], v[32:35]\n\t" \
  "s_add_u32 m0, s66, 0\n\t" \
  "s_nop 0\n\t" \
  "global_load_lds_dwordx4 v240, s[60:61]\n\t" \
  "s_add_u32 m0, s66, 1024\n\t" \
  "s_nop 0\n\t" \
  "global_load_lds_dwordx4 v241, s[60:61]\n\t" \
  "v_mfma_f32_16x16x32_bf16 v[64:67], v[224:227], v[164:167], v[64:67]\n\t" \
  "s_add_u32 m0, s66, 2048\n\t" \
  "s_nop 0\n\t" \
  "global_load_lds_dwordx4 v242, s[60:61]\n\t" \
  "v_mfma_f32_16x16x32_bf16 v[96:99], v[224:227], v[168:171], v[96:99]\n\t" \
  "s_add_u32 m0, s66, 3072\n\t" \
  "s_nop 0\n\t" \
  "global_load_lds_dwordx4 v243, s[60:61]\n\t" \
  "v_mfma_f32_16x16x32_bf16 v[128:131], v[224:227], v[172:175], v[128:131]\n\t" \
  "s_add_u32 s60, s60, 128\n\t" \
  "s_addc_u32 s61, s61, 0\n\t" \
  "v_mfma_f32_16x16x32_bf16 v[36:39], v[228:231], v[160:163], v[36:39]\n\t" \
  "s_add_u32 m0, s67, 32768\n\t" \
  "s_nop 0\n\t" \
  "global_load_lds_dwordx4 v244, s[62:63]\n\t" \
  "s_add_u32 m0, s67, 33792\n\t" \
  "s_nop 0\n\t" \
  "global_load_lds_dwordx4 v245, s[62:63]\n\t" \
  "v_mfma_f32_16x16x32_bf16 v[68:71], v[228:231], v[164:167], v[68:71]\n\t" \
  "s_add_u32 m0, s67, 40960\n\t" \
  "s_nop 0\n\t" \
  "global_load_lds_dwordx4 v246, s[64:65]\n\t" \
  "v_mfma_f32_16x16x32_bf16 v[100:103], v[228:231], v[168:171], v[100:103]\n\t" \
  "s_add_u32 m0, s67, 41984\n\t" \
  "s_nop 0\n\t" \
  "global_load_lds_dwordx4 v247, s[64:65]\n\t" \
  "v_mfma_f32_16x16x32_bf16 v[132:135], v[228:231], v[172:175], v[132:135]\n\t" \
  "s_add_u32 s62, s62, 110592\n\t" \
  "s_addc_u32 s63, s63, 0\n\t" \
  "s_add_u32 s64, s64, 110592\n\t" \
  "s_addc_u32 s65, s65, 0\n\t" \
  "v_mfma_f32_16x16x32_bf16 v[40:43], v[232:235], v[160:163], v[40:43]\n\t" \
  "v_mfma_f32_16x16x32_bf16 v[72:75], v[232:235], v[164:167], v[72:75]\n\t" \
  "v_mfma_f32_16x16x32_bf16 v[104:107], v[232:235], v[168:171], v[104:107]\n\t" \
  "v_mfma_f32_16x16x32_bf16 v[136:139], v[232:235], v[172:175], v[136:139]\n\t" \
  "v_mfma_f32_16x16x32_bf16 v[44:47], v[236:239], v[160:163], v[44:47]\n\t" \
  "v_mfma_f32_16x16x32_bf16 v[76:79], v[236:239], v[164:167], v[76:79]\n\t" \
  "v_mfma_f32_16x16x32_bf16 v[108:111], v[236:239], v[168:171], v[108:111]\n\t" \
  "v_mfma_f32_16x16x32_bf16 v[140:143], v[236:239], v[172:175], v[140:143]\n\t" \
  "s_waitcnt vmcnt(8) lgkmcnt(0)\n\t" \
  "s_barrier\n\t" \
  "v_mfma_f32_16x16x32_bf16 v[16:19], v[176:179], v[144:147], v[16:19]\n\t" \
  "ds_read_b128 v[160:163], v249 offset:16384\n\t" \
  "ds_read_b128 v[164:167], v249 offset:18432\n\t" \
  "v_mfma_f32_16x16x32_bf16 v[48:51], v[176:179], v[148:151], v[48:51]\n\t" \
  "ds_read_b128 v[168:171], v249 offset:20480\n\t" \
  "v_mfma_f32_16x16x32_bf16 v[80:83], v[176:179], v[152:155], v[80:83]\n\t" \
  "ds_read_b128 v[172:175], v249 offset:22528\n\t" \
  "v_mfma_f32_16x16x32_bf16 v[112:115], v[176:179], v[156:159], v[112:115]\n\t" \
  "ds_read_b64_tr_b16 v[208:209], v250 offset:16384\n\t" \
  "v_mfma_f32_16x16x32_bf16 v[20:23], v[180:183], v[144:147], v[20:23]\n\t" \
  "ds_read_b64_tr_b16 v[210:211], v250 offset:17408\n\t" \
  "v_mfma_f32_16x16x32_bf16 v[52:55], v[180:183], v[148:151], v[52:55]\n\t" \
  "ds_read_b64_tr_b16 v[212:213], v250 offset:24576\n\t" \
  "v_mfma_f32_16x16x32_bf16 v[84:87], v[180:183], v[152:155], v[84:87]\n\t" \
  "ds_read_b64_tr_b16 v[214:215], v250 offset:25600\n\t" \
  "v_mfma_f32_16x16x32_bf16 v[116:119], v[180:183], v[156:159], v[116:119]\n\t" \
  "ds_read_b64_tr_b16 v[216:217], v251 offset:16384\n\t" \
  "v_mfma_f32_16x16x32_bf16 v[24:27], v[184:187], v[144:147], v[24:27]\n\t" \
  "ds_read_b64_tr_b16 v[218:219], v251 offset:17408\n\t" \
  "v_mfma_f32_16x16x32_bf16 v[56:59], v[184:187], v[148:151], v[56:59]\n\t" \
  "ds_read_b64_tr_b16 v[220:221], v251 offset:24576\n\t" \
  "v_mfma_f32_16x16x32_bf16 v[88:91], v[184:187], v[152:155], v[88:91]\n\t" \
  "ds_read_b64_tr_b16 v[222:223], v251 offset:25600\n\t" \
  "v_mfma_f32_16x16x32_bf16 v[120:123], v[184:187], v[156:159], v[120:123]\n\t" \
  "ds_read_b64_tr_b16 v[224:225], v252 offset:16384\n\t" \
  "v_mfma_f32_16x16x32_bf16 v[28:31], v[188:191], v[144:147], v[28:31]\n\t" \
  "ds_read_b64_tr_b16 v[226:227], v252 offset:17408\n\t" \
  "v_mfma_f32_16x16x32_bf16 v[60:63], v[188:191], v[148:151], v[60:63]\n\t" \
  "ds_read_b64_tr_b16 v[228:229], v252 offset:24576\n\t" \
  "v_mfma_f32_16x16x32_bf16 v[92:95], v[188:191], v[152:155], v[92:95]\n\t" \
  "ds_read_b64_tr_b16 v[230:231], v252 offset:25600\n\t" \
  "v_mfma_f32_16x16x32_bf16 v[124:127], v[188:191], v[156:159], v[124:127]\n\t" \
  "ds_read_b64_tr_b16 v[232:233], v253 offset:16384\n\t" \
  "v_mfma_f32_16x16x32_bf16 v[32:35], v[192:195], v[144:147], v[32:35]\n\t" \
  "ds_read_b64_tr_b16 v[234:235], v253 offset:17408\n\t" \
  "v_mfma_f32_16x16x32_bf16 v[64:67], v[192:195], v[148:151], v[64:67]\n\t" \
  "ds_read_b64_tr_b16 v[236:237], v253 offset:24576\n\t" \
  "v_mfma_f32_16x16x32_bf16 v[96:99], v[192:195], v[152:155], v[96:99]\n\t" \
  "ds_read_b64_tr_b16 v[238:239], v253 offset:25600\n\t" \
  "v_mfma_f32_16x16x32_bf16 v[128:131], v[192:195], v[156:159], v[128:131]\n\t" \
  "s_add_u32 m0, s67, 0\n\t" \
  "s_nop 0\n\t" \
  "global_load_lds_dwordx4 v244, s[62:63]\n\t" \
  "v_mfma_f32_16x16x32_bf16 v[36:39], v[196:199], v[144:147], v[36:39]\n\t" \
  "s_add_u32 m0, s67, 1024\n\t" \
  "s_nop 0\n\t" \
  "global_load_lds_dwordx4 v245, s[62:63]\n\t" \
  "v_mfma_f32_16x16x32_bf16 v[68:71], v[196:199], v[148:151], v[68:71]\n\t" \
  "s_add_u32 m0, s67, 8192\n\t" \
  "s_nop 0\n\t" \
  "global_load_lds_dwordx4 v246, s[64:65]\n\t" \
  "v_mfma_f32_16x16x32_bf16 v[100:103], v[196:199], v[152:155], v[100:103]\n\t" \
  "s_add_u32 m0, s67, 9216\n\t" \
  "s_nop 0\n\t" \
  "global_load_lds_dwordx4 v247, s[64:65]\n\t" \
  "v_mfma_f32_16x16x32_bf16 v[132:135], v[196:199], v[156:159], v[132:135]\n\t" \
  "s_add_u32 s62, s62, 110592\n\t" \
  "s_addc_u32 s63, s63, 0\n\t" \
  "s_add_u32 s64, s64, 110592\n\t" \
  "s_addc_u32 s65, s65, 0\n\t" \
  "v_mfma_f32_16x16x32_bf16 v[40:43], v[200:203], v[144:147], v[40:43]\n\t" \
  "v_mfma_f32_16x16x32_bf16 v[72:75], v[200:203], v[148:151], v[72:75]\n\t" \
  "v_mfma_f32_16x16x32_bf16 v[104:107], v[200:203], v[152:155], v[104:107]\n\t" \
  "v_mfma_f32_16x16x32_bf16 v[136:139], v[200:203], v[156:159], v[136:139]\n\t" \
  "v_mfma_f32_16x16x32_bf16 v[44:47], v[204:207], v[144:147], v[44:47]\n\t" \
  "v_mfma_f32_16x16x32_bf16 v[76:79], v[204:207], v[148:151], v[76:79]\n\t" \
  "v_mfma_f32_16x16x32_bf16 v[108:111], v[204:207], v[152:155], v[108:111]\n\t" \
  "v_mfma_f32_16x16x32_bf16 v[140:143], v[204:207], v[156:159], v[140:143]\n\t" \
  "s_waitcnt vmcnt(4) lgkmcnt(0)\n\t" \
  "s_barrier\n\t" \
  "v_mfma_f32_16x16x32_bf16 v[16:19], v[208:211], v[160:163], v[16:19]\n\t" \
  "ds_read_b128 v[144:147], v248 offset:0\n\t" \
  "ds_read_b128 v[148:151], v248 offset:2048\n\t" \
  "v_mfma_f32_16x16x32_bf16 v[48:51], v[208:211], v[164:167], v[48:51]\n\t" \
  "ds_read_b128 v[152:155], v248 offset:4096\n\t" \
  "v_mfma_f32_16x16x32_bf16 v[80:83], v[208:211], v[168:171], v[80:83]\n\t" \
  "ds_read_b128 v[156:159], v248 offset:6144\n\t" \
  "v_mfma_f32_16x16x32_bf16 v[112:115], v[208:211], v[172:175], v[112:115]\n\t" \
  "ds_read_b64_tr_b16 v[176:177], v250 offset:32768\n\t" \
  "v_mfma_f32_16x16x32_bf16 v[20:23], v[212:215], v[160:163], v[20:23]\n\t" \
  "ds_read_b64_tr_b16 v[178:179], v250 offset:33792\n\t" \
  "ds_read_b64_tr_b16 v[180:181], v250 offset:40960\n\t" \
  "v_mfma_f32_16x16x32_bf16 v[52:55], v[212:215], v[164:167], v[52:55]\n\t" \
  "ds_read_b64_tr_b16 v[182:183], v250 offset:41984\n\t" \
  "v_mfma_f32_16x16x32_bf16 v[84:87], v[212:215], v[168:171], v[84:87]\n\t" \
  "ds_read_b64_tr_b16 v[184:185], v251 offset:32768\n\t" \
  "v_mfma_f32_16x16x32_bf16 v[116:119], v[212:215], v[172:175], v[116:119]\n\t" \
  "ds_read_b64_tr_b16 v[186:187], v251 offset:33792\n\t" \
  "v_mfma_f32_16x16x32_bf16 v[24:27], v[216:219], v[160:163], v[24:27]\n\t" \
  "ds_read_b64_tr_b16 v[188:189], v251 offset:40960\n\t" \
  "ds_read_b64_tr_b16 v[190:191], v251 offset:41984\n\t" \
  "v_mfma_f32_16x16x32_bf16 v[56:59], v[216:219], v[164:167], v[56:59]\n\t" \
  "ds_read_b64_tr_b16 v[192:193], v252 offset:32768\n\t" \
  "v_mfma_f32_16x16x32_bf16 v[88:91], v[216:219], v[168:171], v[88:91]\n\t" \
  "ds_read_b64_tr_b16 v[194:195], v252 offset:33792\n\t" \
  "v_mfma_f32_16x16x32_bf16 v[120:123], v[216:219], v[172:175], v[120:123]\n\t" \
  "ds_read_b64_tr_b16 v[196:197], v252 offset:40960\n\t" \
  "v_mfma_f32_16x16x32_bf16 v[28:31], v[220:223], v[160:163], v[28:31]\n\t" \
  "ds_read_b64_tr_b16 v[198:199], v252 offset:41984\n\t" \
  "ds_read_b64_tr_b16 v[200:201], v253 offset:32768\n\t" \
  "v_mfma_f32_16x16x32_bf16 v[60:63], v[220:223], v[164:167], v[60:63]\n\t" \
  "ds_read_b64_tr_b16 v[202:203], v253 offset:33792\n\t" \
  "v_mfma_f32_16x16x32_bf16 v[92:95], v[220:223], v[168:171], v[92:95]\n\t" \
  "ds_read_b64_tr_b16 v[204:205], v253 offset:40960\n\t" \
  "v_mfma_f32_16x16x32_bf16 v[124:127], v[220:223], v[172:175], v[124:127]\n\t" \
  "ds_read_b64_tr_b16 v[206:207], v253 offset:41984\n\t" \
  "v_mfma_f32_16x16x32_bf16 v[32:35], v[224:227], v[160:163], v[32:35]\n\t" \
  "s_add_u32 m0, s66, 16384\n\t" \
  "s_nop 0\n\t" \
  "global_load_lds_dwordx4 v240, s[60:61]\n\t" \
  "s_add_u32 m0, s66, 17408\n\t" \
  "s_nop 0\n\t" \
  "global_load_lds_dwordx4 v241, s[60:61]\n\t" \
  "v_mfma_f32_16x16x32_bf16 v[64:67], v[224:227], v[164:167], v[64:67]\n\t" \
  "s_add_u32 m0, s66, 18432\n\t" \
  "s_nop 0\n\t" \
  "global_load_lds_dwordx4 v242, s[60:61]\n\t" \
  "v_mfma_f32_16x16x32_bf16 v[96:99], v[224:227], v[168:171], v[96:99]\n\t" \
  "s_add_u32 m0, s66, 19456\n\t" \
  "s_nop 0\n\t" \
  "global_load_lds_dwordx4 v243, s[60:61]\n\t" \
  "v_mfma_f32_16x16x32_bf16 v[128:131], v[224:227], v[172:175], v[128:131]\n\t" \
  "s_add_u32 s60, s60, 128\n\t" \
  "s_addc_u32 s61, s61, 0\n\t" \
  "v_mfma_f32_16x16x32_bf16 v[36:39], v[228:231], v[160:163], v[36:39]\n\t" \
  "s_add_u32 m0, s67, 16384\n\t" \
  "s_nop 0\n\t" \
  "global_load_lds_dwordx4 v244, s[62:63]\n\t" \
  "s_add_u32 m0, s67, 17408\n\t" \
  "s_nop 0\n\t" \
  "global_load_lds_dwordx4 v245, s[62:63]\n\t" \
  "v_mfma_f32_16x16x32_bf16 v[68:71], v[228:231], v[164:167], v[68:71]\n\t" \
  "s_add_u32 m0, s67, 24576\n\t" \
  "s_nop 0\n\t" \
  "global_load_lds_dwordx4 v246, s[64:65]\n\t" \
  "v_mfma_f32_16x16x32_bf16 v[100:103], v[228:231], v[168:171], v[100:103]\n\t" \
  "s_add_u32 m0, s67, 25600\n\t" \
  "s_nop 0\n\t" \
  "global_load_lds_dwordx4 v247, s[64:65]\n\t" \
  "v_mfma_f32_16x16x32_bf16 v[132:135], v[228:231], v[172:175], v[132:135]\n\t" \
  "s_add_u32 s62, s62, 110592\n\t" \
  "s_addc_u32 s63, s63, 0\n\t" \
  "s_add_u32 s64, s64, 110592\n\t" \
  "s_addc_u32 s65, s65, 0\n\t" \
  "v_mfma_f32_16x16x32_bf16 v[40:43], v[232:235], v[160:163], v[40:43]\n\t" \
  "v_mfma_f32_16x16x32_bf16 v[72:75], v[232:235], v[164:167], v[72:75]\n\t" \
  "v_mfma_f32_16x16x32_bf16 v[104:107], v[232:235], v[168:171], v[104:107]\n\t" \
  "v_mfma_f32_16x16x32_bf16 v[136:139], v[232:235], v[172:175], v[136:139]\n\t" \
  "v_mfma_f32_16x16x32_bf16 v[44:47], v[236:239], v[160:163], v[44:47]\n\t" \
  "v_mfma_f32_16x16x32_bf16 v[76:79], v[236:239], v[164:167], v[76:79]\n\t" \
  "v_mfma_f32_16x16x32_bf16 v[108:111], v[236:239], v[168:171], v[108:111]\n\t" \
  "v_mfma_f32_16x16x32_bf16 v[140:143], v[236:239], v[172:175], v[140:143]\n\t" \
  "s_waitcnt vmcnt(8) lgkmcnt(0)\n\t" \
  "s_barrier\n\t" \
  "v_mfma_f32_16x16x32_bf16 v[16:19], v[176:179], v[144:147], v[16:19]\n\t" \
  "ds_read_b128 v[160:163], v249 offset:0\n\t" \
  "ds_read_b128 v[164:167], v249 offset:2048\n\t" \
  "v_mfma_f32_16x16x32_bf16 v[48:51], v[176:179], v[148:151], v[48:51]\n\t" \
  "ds_read_b128 v[168:171], v249 offset:4096\n\t" \
  "v_mfma_f32_16x16x32_bf16 v[80:83], v[176:179], v[152:155], v[80:83]\n\t" \
  "ds_read_b128 v[172:175], v249 offset:6144\n\t" \
  "v_mfma_f32_16x16x32_bf16 v[112:115], v[176:179], v[156:159], v[112:115]\n\t" \
  "ds_read_b64_tr_b16 v[208:209], v250 offset:0\n\t" \
  "v_mfma_f32_16x16x32_bf16 v[20:23], v[180:183], v[144:147], v[20:23]\n\t" \
  "ds_read_b64_tr_b16 v[210:211], v250 offset:1024\n\t" \
  "v_mfma_f32_16x16x32_bf16 v[52:55], v[180:183], v[148:151], v[52:55]\n\t" \
  "ds_read_b64_tr_b16 v[212:213], v250 offset:8192\n\t" \
  "v_mfma_f32_16x16x32_bf16 v[84:87], v[180:183], v[152:155], v[84:87]\n\t" \
  "ds_read_b64_tr_b16 v[214:215], v250 offset:9216\n\t" \
  "v_mfma_f32_16x16x32_bf16 v[116:119], v[180:183], v[156:159], v[116:119]\n\t" \
  "ds_read_b64_tr_b16 v[216:217], v251 offset:0\n\t" \
  "v_mfma_f32_16x16x32_bf16 v[24:27], v[184:187], v[144:147], v[24:27]\n\t" \
  "ds_read_b64_tr_b16 v[218:219], v251 offset:1024\n\t" \
  "v_mfma_f32_16x16x32_bf16 v[56:59], v[184:187], v[148:151], v[56:59]\n\t" \
  "ds_read_b64_tr_b16 v[220:221], v251 offset:8192\n\t" \
  "v_mfma_f32_16x16x32_bf16 v[88:91], v[184:187], v[152:155], v[88:91]\n\t" \
  "ds_read_b64_tr_b16 v[222:223], v251 offset:9216\n\t" \
  "v_mfma_f32_16x16x32_bf16 v[120:123], v[184:187], v[156:159], v[120:123]\n\t" \
  "ds_read_b64_tr_b16 v[224:225], v252 offset:0\n\t" \
  "v_mfma_f32_16x16x32_bf16 v[28:31], v[188:191], v[144:147], v[28:31]\n\t" \
  "ds_read_b64_tr_b16 v[226:227], v252 offset:1024\n\t" \
  "v_mfma_f32_16x16x32_bf16 v[60:63], v[188:191], v[148:151], v[60:63]\n\t" \
  "ds_read_b64_tr_b16 v[228:229], v252 offset:8192\n\t" \
  "v_mfma_f32_16x16x32_bf16 v[92:95], v[188:191], v[152:155], v[92:95]\n\t" \
  "ds_read_b64_tr_b16 v[230:231], v252 offset:9216\n\t" \
  "v_mfma_f32_16x16x32_bf16 v[124:127], v[188:191], v[156:159], v[124:127]\n\t" \
  "ds_read_b64_tr_b16 v[232:233], v253 offset:0\n\t" \
  "v_mfma_f32_16x16x32_bf16 v[32:35], v[192:195], v[144:147], v[32:35]\n\t" \
  "ds_read_b64_tr_b16 v[234:235], v253 offset:1024\n\t" \
  "v_mfma_f32_16x16x32_bf16 v[64:67], v[192:195], v[148:151], v[64:67]\n\t" \
  "ds_read_b64_tr_b16 v[236:237], v253 offset:8192\n\t" \
  "v_mfma_f32_16x16x32_bf16 v[96:99], v[192:195], v[152:155], v[96:99]\n\t" \
  "ds_read_b64_tr_b16 v[238:239], v253 offset:9216\n\t" \
  "v_mfma_f32_16x16x32_bf16 v[128:131], v[192:195], v[156:159], v[128:131]\n\t" \
  "s_add_u32 m0, s67, 32768\n\t" \
  "s_nop 0\n\t" \
  "global_load_lds_dwordx4 v244, s[62:63]\n\t" \
  "v_mfma_f32_16x16x32_bf16 v[36:39], v[196:199], v[144:147], v[36:39]\n\t" \
  "s_add_u32 m0, s67, 33792\n\t" \
  "s_nop 0\n\t" \
  "global_load_lds_dwordx4 v245, s[62:63]\n\t" \
  "v_mfma_f32_16x16x32_bf16 v[68:71], v[196:199], v[148:151], v[68:71]\n\t" \
  "s_add_u32 m0, s67, 40960\n\t" \
  "s_nop 0\n\t" \
  "global_load_lds_dwordx4 v246, s[64:65]\n\t" \
  "v_mfma_f32_16x16x32_bf16 v[100:103], v[196:199], v[152:155], v[100:103]\n\t" \
  "s_add_u32 m0, s67, 41984\n\t" \
  "s_nop 0\n\t" \
  "global_load_lds_dwordx4 v247, s[64:65]\n\t" \
  "v_mfma_f32_16x16x32_bf16 v[132:135], v[196:199], v[156:159], v[132:135]\n\t" \
  "s_add_u32 s62, s62, 110592\n\t" \
  "s_addc_u32 s63, s63, 0\n\t" \
  "s_add_u32 s64, s64, 110592\n\t" \
  "s_addc_u32 s65, s65, 0\n\t" \
  "v_mfma_f32_16x16x32_bf16 v[40:43], v[200:203], v[144:147], v[40:43]\n\t" \
  "v_mfma_f32_16x16x32_bf16 v[72:75], v[200:203], v[148:151], v[72:75]\n\t" \
  "v_mfma_f32_16x16x32_bf16 v[104:107], v[200:203], v[152:155], v[104:107]\n\t" \
  "v_mfma_f32_16x16x32_bf16 v[136:139], v[200:203], v[156:159], v[136:139]\n\t" \
  "v_mfma_f32_16x16x32_bf16 v[44:47], v[204:207], v[144:147], v[44:47]\n\t" \
  "v_mfma_f32_16x16x32_bf16 v[76:79], v[204:207], v[148:151], v[76:79]\n\t" \
  "v_mfma_f32_16x16x32_bf16 v[108:111], v[204:207], v[152:155], v[108:111]\n\t" \
  "v_mfma_f32_16x16x32_bf16 v[140:143], v[204:207], v[156:159], v[140:143]\n\t" \
  "s_waitcnt vmcnt(4) lgkmcnt(0)\n\t" \
  "s_barrier\n\t" \
  "v_mfma_f32_16x16x32_bf16 v[16:19], v[208:211], v[160:163], v[16:19]\n\t" \
  "ds_read_b128 v[144:147], v248 offset:16384\n\t" \
  "ds_read_b128 v[148:151], v248 offset:18432\n\t" \
  "v_mfma_f32_16x16x32_bf16 v[48:51], v[208:211], v[164:167], v[48:51]\n\t" \
  "ds_read_b128 v[152:155], v248 offset:20480\n\t" \
  "v_mfma_f32_16x16x32_bf16 v[80:83], v[208:211], v[168:171], v[80:83]\n\t" \
  "ds_read_b128 v[156:159], v248 offset:22528\n\t" \
  "v_mfma_f32_16x16x32_bf16 v[112:115], v[208:211], v[172:175], v[112:115]\n\t" \
  "ds_read_b64_tr_b16 v[176:177], v250 offset:16384\n\t" \
  "v_mfma_f32_16x16x32_bf16 v[20:23], v[212:215], v[160:163], v[20:23]\n\t" \
  "ds_read_b64_tr_b16 v[178:179], v250 offset:17408\n\t" \
  "ds_read_b64_tr_b16 v[180:181], v250 offset:24576\n\t" \
  "v_mfma_f32_16x16x32_bf16 v[52:55], v[212:215], v[164:167], v[52:55]\n\t" \
  "ds_read_b64_tr_b16 v[182:183], v250 offset:25600\n\t" \
  "v_mfma_f32_16x16x32_bf16 v[84:87], v[212:215], v[168:171], v[84:87]\n\t" \
  "ds_read_b64_tr_b16 v[184:185], v251 offset:16384\n\t" \
  "v_mfma_f32_16x16x32_bf16 v[116:119], v[212:215], v[172:175], v[116:119]\n\t" \
  "ds_read_b64_tr_b16 v[186:187], v251 offset:17408\n\t" \
  "v_mfma_f32_16x16x32_bf16 v[24:27], v[216:219], v[160:163], v[24:27]\n\t" \
  "ds_read_b64_tr_b16 v[188:189], v251 offset:24576\n\t" \
  "ds_read_b64_tr_b16 v[190:191], v251 offset:25600\n\t" \
  "v_mfma_f32_16x16x32_bf16 v[56:59], v[216:219], v[164:167], v[56:59]\n\t" \
  "ds_read_b64_tr_b16 v[192:193], v252 offset:16384\n\t" \
  "v_mfma_f32_16x16x32_bf16 v[88:91], v[216:219], v[168:171], v[88:91]\n\t" \
  "ds_read_b64_tr_b16 v[194:195], v252 offset:17408\n\t" \
  "v_mfma_f32_16x16x32_bf16 v[120:123], v[216:219], v[172:175], v[120:123]\n\t" \
  "ds_read_b64_tr_b16 v[196:197], v252 offset:24576\n\t" \
  "v_mfma_f32_16x16x32_bf16 v[28:31], v[220:223], v[160:163], v[28:31]\n\t" \
  "ds_read_b64_tr_b16 v[198:199], v252 offset:25600\n\t" \
  "ds_read_b64_tr_b16 v[200:201], v253 offset:16384\n\t" \
  "v_mfma_f32_16x16x32_bf16 v[60:63], v[220:223], v[164:167], v[60:63]\n\t" \
  "ds_read_b64_tr_b16 v[202:203], v253 offset:17408\n\t" \
  "v_mfma_f32_16x16x32_bf16 v[92:95], v[220:223], v[168:171], v[92:95]\n\t" \
  "ds_read_b64_tr_b16 v[204:205], v253 offset:24576\n\t" \
  "v_mfma_f32_16x16x32_bf16 v[124:127], v[220:223], v[172:175], v[124:127]\n\t" \
  "ds_read_b64_tr_b16 v[206:207], v253 offset:25600\n\t" \
  "v_mfma_f32_16x16x32_bf16 v[32:35], v[224:227], v[160:163], v[32:35]\n\t" \
  "s_add_u32 m0, s66, 0\n\t" \
  "s_nop 0\n\t" \
  "global_load_lds_dwordx4 v240, s[60:61]\n\t" \
  "s_add_u32 m0, s66, 1024\n\t" \
  "s_nop 0\n\t" \
  "global_load_lds_dwordx4 v241, s[60:61]\n\t" \
  "v_mfma_f32_16x16x32_bf16 v[64:67], v[224:227], v[164:167], v[64:67]\n\t" \
  "s_add_u32 m0, s66, 2048\n\t" \
  "s_nop 0\n\t" \
  "global_load_lds_dwordx4 v242, s[60:61]\n\t" \
  "v_mfma_f32_16x16x32_bf16 v[96:99], v[224:227], v[168:171], v[96:99]\n\t" \
  "s_add_u32 m0, s66, 3072\n\t" \
  "s_nop 0\n\t" \
  "global_load_lds_dwordx4 v243, s[60:61]\n\t" \
  "v_mfma_f32_16x16x32_bf16 v[128:131], v[224:227], v[172:175], v[128:131]\n\t" \
  "s_add_u32 s60, s60, 128\n\t" \
  "s_addc_u32 s61, s61, 0\n\t" \
  "v_mfma_f32_16x16x32_bf16 v[36:39], v[228:231], v[160:163], v[36:39]\n\t" \
  "s_add_u32 m0, s67, 0\n\t" \
  "s_nop 0\n\t" \
  "global_load_lds_dwordx4 v244, s[62:63]\n\t" \
  "s_add_u32 m0, s67, 1024\n\t" \
  "s_nop 0\n\t" \
  "global_load_lds_dwordx4 v245, s[62:63]\n\t" \
  "v_mfma_f32_16x16x32_bf16 v[68:71], v[228:231], v[164:167], v[68:71]\n\t" \
  "s_add_u32 m0, s67, 8192\n\t" \
  "s_nop 0\n\t" \
  "global_load_lds_dwordx4 v246, s[64:65]\n\t" \
  "v_mfma_f32_16x16x32_bf16 v[100:103], v[228:231], v[168:171], v[100:103]\n\t" \
  "s_add_u32 m0, s67, 9216\n\t" \
  "s_nop 0\n\t" \
  "global_load_lds_dwordx4 v247, s[64:65]\n\t" \
  "v_mfma_f32_16x16x32_bf16 v[132:135], v[228:231], v[172:175], v[132:135]\n\t" \
  "s_add_u32 s62, s62, 110592\n\t" \
  "s_addc_u32 s63, s63, 0\n\t" \
  "s_add_u32 s64, s64, 110592\n\t" \
  "s_addc_u32 s65, s65, 0\n\t" \
  "v_mfma_f32_16x16x32_bf16 v[40:43], v[232:235], v[160:163], v[40:43]\n\t" \
  "v_mfma_f32_16x16x32_bf16 v[72:75], v[232:235], v[164:167], v[72:75]\n\t" \
  "v_mfma_f32_16x16x32_bf16 v[104:107], v[232:235], v[168:171], v[104:107]\n\t" \
  "v_mfma_f32_16x16x32_bf16 v[136:139], v[232:235], v[172:175], v[136:139]\n\t" \
  "v_mfma_f32_16x16x32_bf16 v[44:47], v[236:239], v[160:163], v[44:47]\n\t" \
  "v_mfma_f32_16x16x32_bf16 v[76:79], v[236:239], v[164:167], v[76:79]\n\t" \
  "v_mfma_f32_16x16x32_bf16 v[108:111], v[236:239], v[168:171], v[108:111]\n\t" \
  "v_mfma_f32_16x16x32_bf16 v[140:143], v[236:239], v[172:175], v[140:143]\n\t" \
  "s_setprio %[p2]\n\t" \
  "s_waitcnt vmcnt(8) lgkmcnt(0)\n\t" \
  "s_barrier\n\t" \
  "v_mfma_f32_16x16x32_bf16 v[16:19], v[176:179], v[144:147], v[16:19]\n\t" \
  "ds_read_b128 v[160:163], v249 offset:16384\n\t" \
  "ds_read_b128 v[164:167], v249 offset:18432\n\t" \
  "v_mfma_f32_16x16x32_bf16 v[48:51], v[176:179], v[148:151], v[48:51]\n\t" \
  "ds_read_b128 v[168:171], v249 offset:20480\n\t" \
  "v_mfma_f32_16x16x32_bf16 v[80:83], v[176:179], v[152:155], v[80:83]\n\t" \
  "ds_read_b128 v[172:175], v249 offset:22528\n\t" \
  "v_mfma_f32_16x16x32_bf16 v[112:115], v[176:179], v[156:159], v[112:115]\n\t" \
  "ds_read_b64_tr_b16 v[208:209], v250 offset:32768\n\t" \
  "v_mfma_f32_16x16x32_bf16 v[20:23], v[180:183], v[144:147], v[20:23]\n\t" \
  "ds_read_b64_tr_b16 v[210:211], v250 offset:33792\n\t" \
  "v_mfma_f32_16x16x32_bf16 v[52:55], v[180:183], v[148:151], v[52:55]\n\t" \
  "ds_read_b64_tr_b16 v[212:213], v250 offset:40960\n\t" \
  "v_mfma_f32_16x16x32_bf16 v[84:87], v[180:183], v[152:155], v[84:87]\n\t" \
  "ds_read_b64_tr_b16 v[214:215], v250 offset:41984\n\t" \
  "v_mfma_f32_16x16x32_bf16 v[116:119], v[180:183], v[156:159], v[116:119]\n\t" \
  "ds_read_b64_tr_b16 v[216:217], v251 offset:32768\n\t" \
  "v_mfma_f32_16x16x32_bf16 v[24:27], v[184:187], v[144:147], v[24:27]\n\t" \
  "ds_read_b64_tr_b16 v[218:219], v251 offset:33792\n\t" \
  "v_mfma_f32_16x16x32_bf16 v[56:59], v[184:187], v[148:151], v[56:59]\n\t" \
  "ds_read_b64_tr_b16 v[220:221], v251 offset:40960\n\t" \
  "v_mfma_f32_16x16x32_bf16 v[88:91], v[184:187], v[152:155], v[88:91]\n\t" \
  "ds_read_b64_tr_b16 v[222:223], v251 offset:41984\n\t" \
  "v_mfma_f32_16x16x32_bf16 v[120:123], v[184:187], v[156:159], v[120:123]\n\t" \
  "ds_read_b64_tr_b16 v[224:225], v252 offset:32768\n\t" \
  "v_mfma_f32_16x16x32_bf16 v[28:31], v[188:191], v[144:147], v[28:31]\n\t" \
  "ds_read_b64_tr_b16 v[226:227], v252 offset:33792\n\t" \
  "v_mfma_f32_16x16x32_bf16 v[60:63], v[188:191], v[148:151], v[60:63]\n\t" \
  "ds_read_b64_tr_b16 v[228:229], v252 offset:40960\n\t" \
  "v_mfma_f32_16x16x32_bf16 v[92:95], v[188:191], v[152:155], v[92:95]\n\t" \
  "ds_read_b64_tr_b16 v[230:231], v252 offset:41984\n\t" \
  "v_mfma_f32_16x16x32_bf16 v[124:127], v[188:191], v[156:159], v[124:127]\n\t" \
  "ds_read_b64_tr_b16 v[232:233], v253 offset:32768\n\t" \
  "v_mfma_f32_16x16x32_bf16 v[32:35], v[192:195], v[144:147], v[32:35]\n\t" \
  "ds_read_b64_tr_b16 v[234:235], v253 offset:33792\n\t" \
  "v_mfma_f32_16x16x32_bf16 v[64:67], v[192:195], v[148:151], v[64:67]\n\t" \
  "ds_read_b64_tr_b16 v[236:237], v253 offset:40960\n\t" \
  "v_mfma_f32_16x16x32_bf16 v[96:99], v[192:195], v[152:155], v[96:99]\n\t" \
  "ds_read_b64_tr_b16 v[238:239], v253 offset:41984\n\t" \
  "v_mfma_f32_16x16x32_bf16 v[128:131], v[192:195], v[156:159], v[128:131]\n\t" \
  "s_add_u32 m0, s67, 16384\n\t" \
  "s_nop 0\n\t" \
  "global_load_lds_dwordx4 v244, s[62:63]\n\t" \
  "v_mfma_f32_16x16x32_bf16 v[36:39], v[196:199], v[144:147], v[36:39]\n\t" \
  "s_add_u32 m0, s67, 17408\n\t" \
  "s_nop 0\n\t" \
  "global_load_lds_dwordx4 v245, s[62:63]\n\t" \
  "v_mfma_f32_16x16x32_bf16 v[68:71], v[196:199], v[148:151], v[68:71]\n\t" \
  "s_add_u32 m0, s67, 24576\n\t" \
  "s_nop 0\n\t" \
  "global_load_lds_dwordx4 v246, s[64:65]\n\t" \
  "v_mfma_f32_16x16x32_bf16 v[100:103], v[196:199], v[152:155], v[100:103]\n\t" \
  "s_add_u32 m0, s67, 25600\n\t" \
  "s_nop 0\n\t" \
  "global_load_lds_dwordx4 v247, s[64:65]\n\t" \
  "v_mfma_f32_16x16x32_bf16 v[132:135], v[196:199], v[156:159], v[132:135]\n\t" \
  "s_add_u32 s62, s62, 110592\n\t" \
  "s_addc_u32 s63, s63, 0\n\t" \
  "s_add_u32 s64, s64, 110592\n\t" \
  "s_addc_u32 s65, s65, 0\n\t" \
  "v_mfma_f32_16x16x32_bf16 v[40:43], v[200:203], v[144:147], v[40:43]\n\t" \
  "v_mfma_f32_16x16x32_bf16 v[72:75], v[200:203], v[148:151], v[72:75]\n\t" \
  "v_mfma_f32_16x16x32_bf16 v[104:107], v[200:203], v[152:155], v[104:107]\n\t" \
  "v_mfma_f32_16x16x32_bf16 v[136:139], v[200:203], v[156:159], v[136:139]\n\t" \
  "v_mfma_f32_16x16x32_bf16 v[44:47], v[204:207], v[144:147], v[44:47]\n\t" \
  "v_mfma_f32_16x16x32_bf16 v[76:79], v[204:207], v[148:151], v[76:79]\n\t" \
  "v_mfma_f32_16x16x32_bf16 v[108:111], v[204:207], v[152:155], v[108:111]\n\t" \
  "v_mfma_f32_16x16x32_bf16 v[140:143], v[204:207], v[156:159], v[140:143]\n\t" \
  "s_waitcnt vmcnt(4) lgkmcnt(0)\n\t" \
  "s_barrier\n\t" \
  "v_mfma_f32_16x16x32_bf16 v[16:19], v[208:211], v[160:163], v[16:19]\n\t" \
  "ds_read_b128 v[144:147], v248 offset:0\n\t" \
  "ds_read_b128 v[148:151], v248 offset:2048\n\t" \
  "v_mfma_f32_16x16x32_bf16 v[48:51], v[208:211], v[164:167], v[48:51]\n\t" \
  "ds_read_b128 v[152:155], v248 offset:4096\n\t" \
  "v_mfma_f32_16x16x32_bf16 v[80:83], v[208:211], v[168:171], v[80:83]\n\t" \
  "ds_read_b128 v[156:159], v248 offset:6144\n\t" \
  "v_mfma_f32_16x16x32_bf16 v[112:115], v[208:211], v[172:175], v[112:115]\n\t" \
  "ds_read_b64_tr_b16 v[176:177], v250 offset:0\n\t" \
  "v_mfma_f32_16x16x32_bf16 v[20:23], v[212:215], v[160:163], v[20:23]\n\t" \
  "ds_read_b64_tr_b16 v[178:179], v250 offset:1024\n\t" \
  "ds_read_b64_tr_b16 v[180:181], v250 offset:8192\n\t" \
  "v_mfma_f32_16x16x32_bf16 v[52:55], v[212:215], v[164:167], v[52:55]\n\t" \
  "ds_read_b64_tr_b16 v[182:183], v250 offset:9216\n\t" \
  "v_mfma_f32_16x16x32_bf16 v[84:87], v[212:215], v[168:171], v[84:87]\n\t" \
  "ds_read_b64_tr_b16 v[184:185], v251 offset:0\n\t" \
  "v_mfma_f32_16x16x32_bf16 v[116:119], v[212:215], v[172:175], v[116:119]\n\t" \
  "ds_read_b64_tr_b16 v[186:187], v251 offset:1024\n\t" \
  "v_mfma_f32_16x16x32_bf16 v[24:27], v[216:219], v[160:163], v[24:27]\n\t" \
  "ds_read_b64_tr_b16 v[188:189], v251 offset:8192\n\t" \
  "ds_read_b64_tr_b16 v[190:191], v251 offset:9216\n\t" \
  "v_mfma_f32_16x16x32_bf16 v[56:59], v[216:219], v[164:167], v[56:59]\n\t" \
  "ds_read_b64_tr_b16 v[192:193], v252 offset:0\n\t" \
  "v_mfma_f32_16x16x32_bf16 v[88:91], v[216:219], v[168:171], v[88:91]\n\t" \
  "ds_read_b64_tr_b16 v[194:195], v252 offset:1024\n\t" \
  "v_mfma_f32_16x16x32_bf16 v[120:123], v[216:219], v[172:175], v[120:123]\n\t" \
  "ds_read_b64_tr_b16 v[196:197], v252 offset:8192\n\t" \
  "v_mfma_f32_16x16x32_bf16 v[28:31], v[220:223], v[160:163], v[28:31]\n\t" \
  "ds_read_b64_tr_b16 v[198:199], v252 offset:9216\n\t" \
  "ds_read_b64_tr_b16 v[200:201], v253 offset:0\n\t" \
  "v_mfma_f32_16x16x32_bf16 v[60:63], v[220:223], v[164:167], v[60:63]\n\t" \
  "ds_read_b64_tr_b16 v[202:203], v253 offset:1024\n\t" \
  "v_mfma_f32_16x16x32_bf16 v[92:95], v[220:223], v[168:171], v[92:95]\n\t" \
  "ds_read_b64_tr_b16 v[204:205], v253 offset:8192\n\t" \
  "v_mfma_f32_16x16x32_bf16 v[124:127], v[220:223], v[172:175], v[124:127]\n\t" \
  "ds_read_b64_tr_b16 v[206:207], v253 offset:9216\n\t" \
  "v_mfma_f32_16x16x32_bf16 v[32:35], v[224:227], v[160:163], v[32:35]\n\t" \
  "s_add_u32 m0, s66, 16384\n\t" \
  "s_nop 0\n\t" \
  "global_load_lds_dwordx4 v240, s[60:61]\n\t" \
  "s_add_u32 m0, s66, 17408\n\t" \
  "s_nop 0\n\t" \
  "global_load_lds_dwordx4 v241, s[60:61]\n\t" \
  "v_mfma_f32_16x16x32_bf16 v[64:67], v[224:227], v[164:167], v[64:67]\n\t" \
  "s_add_u32 m0, s66, 18432\n\t" \
  "s_nop 0\n\t" \
  "global_load_lds_dwordx4 v242, s[60:61]\n\t" \
  "v_mfma_f32_16x16x32_bf16 v[96:99], v[224:227], v[168:171], v[96:99]\n\t" \
  "s_add_u32 m0, s66, 19456\n\t" \
  "s_nop 0\n\t" \
  "global_load_lds_dwordx4 v243, s[60:61]\n\t" \
  "v_mfma_f32_16x16x32_bf16 v[128:131], v[224:227], v[172:175], v[128:131]\n\t" \
  "s_add_u32 s60, s60, 128\n\t" \
  "s_addc_u32 s61, s61, 0\n\t" \
  "v_mfma_f32_16x16x32_bf16 v[36:39], v[228:231], v[160:163], v[36:39]\n\t" \
  "s_add_u32 m0, s67, 32768\n\t" \
  "s_nop 0\n\t" \
  "global_load_lds_dwordx4 v244, s[62:63]\n\t" \
  "s_add_u32 m0, s67, 33792\n\t" \
  "s_nop 0\n\t" \
  "global_load_lds_dwordx4 v245, s[62:63]\n\t" \
  "v_mfma_f32_16x16x32_bf16 v[68:71], v[228:231], v[164:167], v[68:71]\n\t" \
  "s_add_u32 m0, s67, 40960\n\t" \
  "s_nop 0\n\t" \
  "global_load_lds_dwordx4 v246, s[64:65]\n\t" \
  "v_mfma_f32_16x16x32_bf16 v[100:103], v[228:231], v[168:171], v[100:103]\n\t" \
  "s_add_u32 m0, s67, 41984\n\t" \
  "s_nop 0\n\t" \
  "global_load_lds_dwordx4 v247, s[64:65]\n\t" \
  "v_mfma_f32_16x16x32_bf16 v[132:135], v[228:231], v[172:175], v[132:135]\n\t" \
  "s_add_u32 s62, s62, 110592\n\t" \
  "s_addc_u32 s63, s63, 0\n\t" \
  "s_add_u32 s64, s64, 110592\n\t" \
  "s_addc_u32 s65, s65, 0\n\t" \
  "v_mfma_f32_16x16x32_bf16 v[40:43], v[232:235], v[160:163], v[40:43]\n\t" \
  "v_mfma_f32_16x16x32_bf16 v[72:75], v[232:235], v[164:167], v[72:75]\n\t" \
  "v_mfma_f32_16x16x32_bf16 v[104:107], v[232:235], v[168:171], v[104:107]\n\t" \
  "v_mfma_f32_16x16x32_bf16 v[136:139], v[232:235], v[172:175], v[136:139]\n\t" \
  "v_mfma_f32_16x16x32_bf16 v[44:47], v[236:239], v[160:163], v[44:47]\n\t" \
  "v_mfma_f32_16x16x32_bf16 v[76:79], v[236:239], v[164:167], v[76:79]\n\t" \
  "v_mfma_f32_16x16x32_bf16 v[108:111], v[236:239], v[168:171], v[108:111]\n\t" \
  "v_mfma_f32_16x16x32_bf16 v[140:143], v[236:239], v[172:175], v[140:143]\n\t" \
  "s_waitcnt vmcnt(8) lgkmcnt(0)\n\t" \
  "s_barrier\n\t" \
  "v_mfma_f32_16x16x32_bf16 v[16:19], v[176:179], v[144:147], v[16:19]\n\t" \
  "ds_read_b128 v[160:163], v249 offset:0\n\t" \
  "ds_read_b128 v[164:167], v249 offset:2048\n\t" \
  "v_mfma_f32_16x16x32_bf16 v[48:51], v[176:179], v[148:151], v[48:51]\n\t" \
  "ds_read_b128 v[168:171], v249 offset:4096\n\t" \
  "v_mfma_f32_16x16x32_bf16 v[80:83], v[176:179], v[152:155], v[80:83]\n\t" \
  "ds_read_b128 v[172:175], v249 offset:6144\n\t" \
  "v_mfma_f32_16x16x32_bf16 v[112:115], v[176:179], v[156:159], v[112:115]\n\t" \
  "ds_read_b64_tr_b16 v[208:209], v250 offset:16384\n\t" \
  "v_mfma_f32_16x16x32_bf16 v[20:23], v[180:183], v[144:147], v[20:23]\n\t" \
  "ds_read_b64_tr_b16 v[210:211], v250 offset:17408\n\t" \
  "v_mfma_f32_16x16x32_bf16 v[52:55], v[180:183], v[148:151], v[52:55]\n\t" \
  "ds_read_b64_tr_b16 v[212:213], v250 offset:24576\n\t" \
  "v_mfma_f32_16x16x32_bf16 v[84:87], v[180:183], v[152:155], v[84:87]\n\t" \
  "ds_read_b64_tr_b16 v[214:215], v250 offset:25600\n\t" \
  "v_mfma_f32_16x16x32_bf16 v[116:119], v[180:183], v[156:159], v[116:119]\n\t" \
  "ds_read_b64_tr_b16 v[216:217], v251 offset:16384\n\t" \
  "v_mfma_f32_16x16x32_bf16 v[24:27], v[184:187], v[144:147], v[24:27]\n\t" \
  "ds_read_b64_tr_b16 v[218:219], v251 offset:17408\n\t" \
  "v_mfma_f32_16x16x32_bf16 v[56:59], v[184:187], v[148:151], v[56:59]\n\t" \
  "ds_read_b64_tr_b16 v[220:221], v251 offset:24576\n\t" \
  "v_mfma_f32_16x16x32_bf16 v[88:91], v[184:187], v[152:155], v[88:91]\n\t" \
  "ds_read_b64_tr_b16 v[222:223], v251 offset:25600\n\t" \
  "v_mfma_f32_16x16x32_bf16 v[120:123], v[184:187], v[156:159], v[120:123]\n\t" \
  "ds_read_b64_tr_b16 v[224:225], v252 offset:16384\n\t" \
  "v_mfma_f32_16x16x32_bf16 v[28:31], v[188:191], v[144:147], v[28:31]\n\t" \
  "ds_read_b64_tr_b16 v[226:227], v252 offset:17408\n\t" \
  "v_mfma_f32_16x16x32_bf16 v[60:63], v[188:191], v[148:151], v[60:63]\n\t" \
  "ds_read_b64_tr_b16 v[228:229], v252 offset:24576\n\t" \
  "v_mfma_f32_16x16x32_bf16 v[92:95], v[188:191], v[152:155], v[92:95]\n\t" \
  "ds_read_b64_tr_b16 v[230:231], v252 offset:25600\n\t" \
  "v_mfma_f32_16x16x32_bf16 v[124:127], v[188:191], v[156:159], v[124:127]\n\t" \
  "ds_read_b64_tr_b16 v[232:233], v253 offset:16384\n\t" \
  "v_mfma_f32_16x16x32_bf16 v[32:35], v[192:195], v[144:147], v[32:35]\n\t" \
  "ds_read_b64_tr_b16 v[234:235], v253 offset:17408\n\t" \
  "v_mfma_f32_16x16x32_bf16 v[64:67], v[192:195], v[148:151], v[64:67]\n\t" \
  "ds_read_b64_tr_b16 v[236:237], v253 offset:24576\n\t" \
  "v_mfma_f32_16x16x32_bf16 v[96:99], v[192:195], v[152:155], v[96:99]\n\t" \
  "ds_read_b64_tr_b16 v[238:239], v253 offset:25600\n\t" \
  "v_mfma_f32_16x16x32_bf16 v[128:131], v[192:195], v[156:159], v[128:131]\n\t" \
  "s_add_u32 m0, s67, 0\n\t" \
  "s_nop 0\n\t" \
  "global_load_lds_dwordx4 v244, s[62:63]\n\t" \
  "v_mfma_f32_16x16x32_bf16 v[36:39], v[196:199], v[144:147], v[36:39]\n\t" \
  "s_add_u32 m0, s67, 1024\n\t" \
  "s_nop 0\n\t" \
  "global_load_lds_dwordx4 v245, s[62:63]\n\t" \
  "v_mfma_f32_16x16x32_bf16 v[68:71], v[196:199], v[148:151], v[68:71]\n\t" \
  "s_add_u32 m0, s67, 8192\n\t" \
  "s_nop 0\n\t" \
  "global_load_lds_dwordx4 v246, s[64:65]\n\t" \
  "v_mfma_f32_16x16x32_bf16 v[100:103], v[196:199], v[152:155], v[100:103]\n\t" \
  "s_add_u32 m0, s67, 9216\n\t" \
  "s_nop 0\n\t" \
  "global_load_lds_dwordx4 v247, s[64:65]\n\t" \
  "v_mfma_f32_16x16x32_bf16 v[132:135], v[196:199], v[156:159], v[132:135]\n\t" \
  "s_add_u32 s62, s62, 110592\n\t" \
  "s_addc_u32 s63, s63, 0\n\t" \
  "s_add_u32 s64, s64, 110592\n\t" \
  "s_addc_u32 s65, s65, 0\n\t" \
  "v_mfma_f32_16x16x32_bf16 v[40:43], v[200:203], v[144:147], v[40:43]\n\t" \
  "v_mfma_f32_16x16x32_bf16 v[72:75], v[200:203], v[148:151], v[72:75]\n\t" \
  "v_mfma_f32_16x16x32_bf16 v[104:107], v[200:203], v[152:155], v[104:107]\n\t" \
  "v_mfma_f32_16x16x32_bf16 v[136:139], v[200:203], v[156:159], v[136:139]\n\t" \
  "v_mfma_f32_16x16x32_bf16 v[44:47], v[204:207], v[144:147], v[44:47]\n\t" \
  "v_mfma_f32_16x16x32_bf16 v[76:79], v[204:207], v[148:151], v[76:79]\n\t" \
  "v_mfma_f32_16x16x32_bf16 v[108:111], v[204:207], v[152:155], v[108:111]\n\t" \
  "v_mfma_f32_16x16x32_bf16 v[140:143], v[204:207], v[156:159], v[140:143]\n\t" \
  "s_waitcnt vmcnt(4) lgkmcnt(0)\n\t" \
  "s_barrier\n\t" \
  "v_mfma_f32_16x16x32_bf16 v[16:19], v[208:211], v[160:163], v[16:19]\n\t" \
  "ds_read_b128 v[144:147], v248 offset:16384\n\t" \
  "ds_read_b128 v[148:151], v248 offset:18432\n\t" \
  "v_mfma_f32_16x16x32_bf16 v[48:51], v[208:211], v[164:167], v[48:51]\n\t" \
  "ds_read_b128 v[152:155], v248 offset:20480\n\t" \
  "v_mfma_f32_16x16x32_bf16 v[80:83], v[208:211], v[168:171], v[80:83]\n\t" \
  "ds_read_b128 v[156:159], v248 offset:22528\n\t" \
  "v_mfma_f32_16x16x32_bf16 v[112:115], v[208:211], v[172:175], v[112:115]\n\t" \
  "ds_read_b64_tr_b16 v[176:177], v250 offset:32768\n\t" \
  "v_mfma_f32_16x16x32_bf16 v[20:23], v[212:215], v[160:163], v[20:23]\n\t" \
  "ds_read_b64_tr_b16 v[178:179], v250 offset:33792\n\t" \
  "ds_read_b64_tr_b16 v[180:181], v250 offset:40960\n\t" \
  "v_mfma_f32_16x16x32_bf16 v[52:55], v[212:215], v[164:167], v[52:55]\n\t" \
  "ds_read_b64_tr_b16 v[182:183], v250 offset:41984\n\t" \
  "v_mfma_f32_16x16x32_bf16 v[84:87], v[212:215], v[168:171], v[84:87]\n\t" \
  "ds_read_b64_tr_b16 v[184:185], v251 offset:32768\n\t" \
  "v_mfma_f32_16x16x32_bf16 v[116:119], v[212:215], v[172:175], v[116:119]\n\t" \
  "ds_read_b64_tr_b16 v[186:187], v251 offset:33792\n\t" \
  "v_mfma_f32_16x16x32_bf16 v[24:27], v[216:219], v[160:163], v[24:27]\n\t" \
  "ds_read_b64_tr_b16 v[188:189], v251 offset:40960\n\t" \
  "ds_read_b64_tr_b16 v[190:191], v251 offset:41984\n\t" \
  "v_mfma_f32_16x16x32_bf16 v[56:59], v[216:219], v[164:167], v[56:59]\n\t" \
  "ds_read_b64_tr_b16 v[192:193], v252 offset:32768\n\t" \
  "v_mfma_f32_16x16x32_bf16 v[88:91], v[216:219], v[168:171], v[88:91]\n\t" \
  "ds_read_b64_tr_b16 v[194:195], v252 offset:33792\n\t" \
  "v_mfma_f32_16x16x32_bf16 v[120:123], v[216:219], v[172:175], v[120:123]\n\t" \
  "ds_read_b64_tr_b16 v[196:197], v252 offset:40960\n\t" \
  "v_mfma_f32_16x16x32_bf16 v[28:31], v[220:223], v[160:163], v[28:31]\n\t" \
  "ds_read_b64_tr_b16 v[198:199], v252 offset:41984\n\t" \
  "ds_read_b64_tr_b16 v[200:201], v253 offset:32768\n\t" \
  "v_mfma_f32_16x16x32_bf16 v[60:63], v[220:223], v[164:167], v[60:63]\n\t" \
  "ds_read_b64_tr_b16 v[202:203], v253 offset:33792\n\t" \
  "v_mfma_f32_16x16x32_bf16 v[92:95], v[220:223], v[168:171], v[92:95]\n\t" \
  "ds_read_b64_tr_b16 v[204:205], v253 offset:40960\n\t" \
  "v_mfma_f32_16x16x32_bf16 v[124:127], v[220:223], v[172:175], v[124:127]\n\t" \
  "ds_read_b64_tr_b16 v[206:207], v253 offset:41984\n\t" \
  "v_mfma_f32_16x16x32_bf16 v[32:35], v[224:227], v[160:163], v[32:35]\n\t" \
  "s_add_u32 m0, s66, 0\n\t" \
  "s_nop 0\n\t" \
  "global_load_lds_dwordx4 v240, s[60:61]\n\t" \
  "s_add_u32 m0, s66, 1024\n\t" \
  "s_nop 0\n\t" \
  "global_load_lds_dwordx4 v241, s[60:61]\n\t" \
  "v_mfma_f32_16x16x32_bf16 v[64:67], v[224:227], v[164:167], v[64:67]\n\t" \
  "s_add_u32 m0, s66, 2048\n\t" \
  "s_nop 0\n\t" \
  "global_load_lds_dwordx4 v242, s[60:61]\n\t" \
  "v_mfma_f32_16x16x32_bf16 v[96:99], v[224:227], v[168:171], v[96:99]\n\t" \
  "s_add_u32 m0, s66, 3072\n\t" \
  "s_nop 0\n\t" \
  "global_load_lds_dwordx4 v243, s[60:61]\n\t" \
  "v_mfma_f32_16x16x32_bf16 v[128:131], v[224:227], v[172:175], v[128:131]\n\t" \
  "s_add_u32 s60, s60, 128\n\t" \
  "s_addc_u32 s61, s61, 0\n\t" \
  "v_mfma_f32_16x16x32_bf16 v[36:39], v[228:231], v[160:163], v[36:39]\n\t" \
  "s_add_u32 m0, s67, 16384\n\t" \
  "s_nop 0\n\t" \
  "global_load_lds_dwordx4 v244, s[62:63]\n\t" \
  "s_add_u32 m0, s67, 17408\n\t" \
  "s_nop 0\n\t" \
  "global_load_lds_dwordx4 v245, s[62:63]\n\t" \
  "v_mfma_f32_16x16x32_bf16 v[68:71], v[228:231], v[164:167], v[68:71]\n\t" \
  "s_add_u32 m0, s67, 24576\n\t" \
  "s_nop 0\n\t" \
  "global_load_lds_dwordx4 v246, s[64:65]\n\t" \
  "v_mfma_f32_16x16x32_bf16 v[100:103], v[228:231], v[168:171], v[100:103]\n\t" \
  "s_add_u32 m0, s67, 25600\n\t" \
  "s_nop 0\n\t" \
  "global_load_lds_dwordx4 v247, s[64:65]\n\t" \
  "v_mfma_f32_16x16x32_bf16 v[132:135], v[228:231], v[172:175], v[132:135]\n\t" \
  "s_add_u32 s62, s62, 110592\n\t" \
  "s_addc_u32 s63, s63, 0\n\t" \
  "s_add_u32 s64, s64, 110592\n\t" \
  "s_addc_u32 s65, s65, 0\n\t" \
  "v_mfma_f32_16x16x32_bf16 v[40:43], v[232:235], v[160:163], v[40:43]\n\t" \
  "v_mfma_f32_16x16x32_bf16 v[72:75], v[232:235], v[164:167], v[72:75]\n\t" \
  "v_mfma_f32_16x16x32_bf16 v[104:107], v[232:235], v[168:171], v[104:107]\n\t" \
  "v_mfma_f32_16x16x32_bf16 v[136:139], v[232:235], v[172:175], v[136:139]\n\t" \
  "v_mfma_f32_16x16x32_bf16 v[44:47], v[236:239], v[160:163], v[44:47]\n\t" \
  "v_mfma_f32_16x16x32_bf16 v[76:79], v[236:239], v[164:167], v[76:79]\n\t" \
  "v_mfma_f32_16x16x32_bf16 v[108:111], v[236:239], v[168:171], v[108:111]\n\t" \
  "v_mfma_f32_16x16x32_bf16 v[140:143], v[236:239], v[172:175], v[140:143]\n\t" \
  "s_waitcnt vmcnt(8) lgkmcnt(0)\n\t" \
  "s_barrier\n\t" \
  "v_mfma_f32_16x16x32_bf16 v[16:19], v[176:179], v[144:147], v[16:19]\n\t" \
  "ds_read_b128 v[160:163], v249 offset:16384\n\t" \
  "ds_read_b128 v[164:167], v249 offset:18432\n\t" \
  "v_mfma_f32_16x16x32_bf16 v[48:51], v[176:179], v[148:151], v[48:51]\n\t" \
  "ds_read_b128 v[168:171], v249 offset:20480\n\t" \
  "v_mfma_f32_16x16x32_bf16 v[80:83], v[176:179], v[152:155], v[80:83]\n\t" \
  "ds_read_b128 v[172:175], v249 offset:22528\n\t" \
  "v_mfma_f32_16x16x32_bf16 v[112:115], v[176:179], v[156:159], v[112:115]\n\t" \
  "ds_read_b64_tr_b16 v[208:209], v250 offset:0\n\t" \
  "v_mfma_f32_16x16x32_bf16 v[20:23], v[180:183], v[144:147], v[20:23]\n\t" \
  "ds_read_b64_tr_b16 v[210:211], v250 offset:1024\n\t" \
  "v_mfma_f32_16x16x32_bf16 v[52:55], v[180:183], v[148:151], v[52:55]\n\t" \
  "ds_read_b64_tr_b16 v[212:213], v250 offset:8192\n\t" \
  "v_mfma_f32_16x16x32_bf16 v[84:87], v[180:183], v[152:155], v[84:87]\n\t" \
  "ds_read_b64_tr_b16 v[214:215], v250 offset:9216\n\t" \
  "v_mfma_f32_16x16x32_bf16 v[116:119], v[180:183], v[156:159], v[116:119]\n\t" \
  "ds_read_b64_tr_b16 v[216:217], v251 offset:0\n\t" \
  "v_mfma_f32_16x16x32_bf16 v[24:27], v[184:187], v[144:147], v[24:27]\n\t" \
  "ds_read_b64_tr_b16 v[218:219], v251 offset:1024\n\t" \
  "v_mfma_f32_16x16x32_bf16 v[56:59], v[184:187], v[148:151], v[56:59]\n\t" \
  "ds_read_b64_tr_b16 v[220:221], v251 offset:8192\n\t" \
  "v_mfma_f32_16x16x32_bf16 v[88:91], v[184:187], v[152:155], v[88:91]\n\t" \
  "ds_read_b64_tr_b16 v[222:223], v251 offset:9216\n\t" \
  "v_mfma_f32_16x16x32_bf16 v[120:123], v[184:187], v[156:159], v[120:123]\n\t" \
  "ds_read_b64_tr_b16 v[224:225], v252 offset:0\n\t" \
  "v_mfma_f32_16x16x32_bf16 v[28:31], v[188:191], v[144:147], v[28:31]\n\t" \
  "ds_read_b64_tr_b16 v[226:227], v252 offset:1024\n\t" \
  "v_mfma_f32_16x16x32_bf16 v[60:63], v[188:191], v[148:151], v[60:63]\n\t" \
  "ds_read_b64_tr_b16 v[228:229], v252 offset:8192\n\t" \
  "v_mfma_f32_16x16x32_bf16 v[92:95], v[188:191], v[152:155], v[92:95]\n\t" \
  "ds_read_b64_tr_b16 v[230:231], v252 offset:9216\n\t" \
  "v_mfma_f32_16x16x32_bf16 v[124:127], v[188:191], v[156:159], v[124:127]\n\t" \
  "ds_read_b64_tr_b16 v[232:233], v253 offset:0\n\t" \
  "v_mfma_f32_16x16x32_bf16 v[32:35], v[192:195], v[144:147], v[32:35]\n\t" \
  "ds_read_b64_tr_b16 v[234:235], v253 offset:1024\n\t" \
  "v_mfma_f32_16x16x32_bf16 v[64:67], v[192:195], v[148:151], v[64:67]\n\t" \
  "ds_read_b64_tr_b16 v[236:237], v253 offset:8192\n\t" \
  "v_mfma_f32_16x16x32_bf16 v[96:99], v[192:195], v[152:155], v[96:99]\n\t" \
  "ds_read_b64_tr_b16 v[238:239], v253 offset:9216\n\t" \
  "v_mfma_f32_16x16x32_bf16 v[128:131], v[192:195], v[156:159], v[128:131]\n\t" \
  "s_add_u32 m0, s67, 32768\n\t" \
  "s_nop 0\n\t" \
  "global_load_lds_dwordx4 v244, s[62:63]\n\t" \
  "v_mfma_f32_16x16x32_bf16 v[36:39], v[196:199], v[144:147], v[36:39]\n\t" \
  "s_add_u32 m0, s67, 33792\n\t" \
  "s_nop 0\n\t" \
  "global_load_lds_dwordx4 v245, s[62:63]\n\t" \
  "v_mfma_f32_16x16x32_bf16 v[68:71], v[196:199], v[148:151], v[68:71]\n\t" \
  "s_add_u32 m0, s67, 40960\n\t" \
  "s_nop 0\n\t" \
  "global_load_lds_dwordx4 v246, s[64:65]\n\t" \
  "v_mfma_f32_16x16x32_bf16 v[100:103], v[196:199], v[152:155], v[100:103]\n\t" \
  "s_add_u32 m0, s67, 41984\n\t" \
  "s_nop 0\n\t" \
  "global_load_lds_dwordx4 v247, s[64:65]\n\t" \
  "v_mfma_f32_16x16x32_bf16 v[132:135], v[196:199], v[156:159], v[132:135]\n\t" \
  "s_add_u32 s62, s62, 110592\n\t" \
  "s_addc_u32 s63, s63, 0\n\t" \
  "s_add_u32 s64, s64, 110592\n\t" \
  "s_addc_u32 s65, s65, 0\n\t" \
  "v_mfma_f32_16x16x32_bf16 v[40:43], v[200:203], v[144:147], v[40:43]\n\t" \
  "v_mfma_f32_16x16x32_bf16 v[72:75], v[200:203], v[148:151], v[72:75]\n\t" \
  "v_mfma_f32_16x16x32_bf16 v[104:107], v[200:203], v[152:155], v[104:107]\n\t" \
  "v_mfma_f32_16x16x32_bf16 v[136:139], v[200:203], v[156:159], v[136:139]\n\t" \
  "v_mfma_f32_16x16x32_bf16 v[44:47], v[204:207], v[144:147], v[44:47]\n\t" \
  "v_mfma_f32_16x16x32_bf16 v[76:79], v[204:207], v[148:151], v[76:79]\n\t" \
  "v_mfma_f32_16x16x32_bf16 v[108:111], v[204:207], v[152:155], v[108:111]\n\t" \
  "v_mfma_f32_16x16x32_bf16 v[140:143], v[204:207], v[156:159], v[140:143]\n\t" \
  "s_waitcnt vmcnt(4) lgkmcnt(0)\n\t" \
  "s_barrier\n\t" \
  "v_mfma_f32_16x16x32_bf16 v[16:19], v[208:211], v[160:163], v[16:19]\n\t" \
  "ds_read_b128 v[144:147], v248 offset:0\n\t" \
  "ds_read_b128 v[148:151], v248 offset:2048\n\t" \
  "v_mfma_f32_16x16x32_bf16 v[48:51], v[208:211], v[164:167], v[48:51]\n\t" \
  "ds_read_b128 v[152:155], v248 offset:4096\n\t" \
  "v_mfma_f32_16x16x32_bf16 v[80:83], v[208:211], v[168:171], v[80:83]\n\t" \
  "ds_read_b128 v[156:159], v248 offset:6144\n\t" \
  "v_mfma_f32_16x16x32_bf16 v[112:115], v[208:211], v[172:175], v[112:115]\n\t" \
  "ds_read_b64_tr_b16 v[176:177], v250 offset:16384\n\t" \
  "v_mfma_f32_16x16x32_bf16 v[20:23], v[212:215], v[160:163], v[20:23]\n\t" \
  "ds_read_b64_tr_b16 v[178:179], v250 offset:17408\n\t" \
  "ds_read_b64_tr_b16 v[180:181], v250 offset:24576\n\t" \
  "v_mfma_f32_16x16x32_bf16 v[52:55], v[212:215], v[164:167], v[52:55]\n\t" \
  "ds_read_b64_tr_b16 v[182:183], v250 offset:25600\n\t" \
  "v_mfma_f32_16x16x32_bf16 v[84:87], v[212:215], v[168:171], v[84:87]\n\t" \
  "ds_read_b64_tr_b16 v[184:185], v251 offset:16384\n\t" \
  "v_mfma_f32_16x16x32_bf16 v[116:119], v[212:215], v[172:175], v[116:119]\n\t" \
  "ds_read_b64_tr_b16 v[186:187], v251 offset:17408\n\t" \
  "v_mfma_f32_16x16x32_bf16 v[24:27], v[216:219], v[160:163], v[24:27]\n\t" \
  "ds_read_b64_tr_b16 v[188:189], v251 offset:24576\n\t" \
  "ds_read_b64_tr_b16 v[190:191], v251 offset:25600\n\t" \
  "v_mfma_f32_16x16x32_bf16 v[56:59], v[216:219], v[164:167], v[56:59]\n\t" \
  "ds_read_b64_tr_b16 v[192:193], v252 offset:16384\n\t" \
  "v_mfma_f32_16x16x32_bf16 v[88:91], v[216:219], v[168:171], v[88:91]\n\t" \
  "ds_read_b64_tr_b16 v[194:195], v252 offset:17408\n\t" \
  "v_mfma_f32_16x16x32_bf16 v[120:123], v[216:219], v[172:175], v[120:123]\n\t" \
  "ds_read_b64_tr_b16 v[196:197], v252 offset:24576\n\t" \
  "v_mfma_f32_16x16x32_bf16 v[28:31], v[220:223], v[160:163], v[28:31]\n\t" \
  "ds_read_b64_tr_b16 v[198:199], v252 offset:25600\n\t" \
  "ds_read_b64_tr_b16 v[200:201], v253 offset:16384\n\t" \
  "v_mfma_f32_16x16x32_bf16 v[60:63], v[220:223], v[164:167], v[60:63]\n\t" \
  "ds_read_b64_tr_b16 v[202:203], v253 offset:17408\n\t" \
  "v_mfma_f32_16x16x32_bf16 v[92:95], v[220:223], v[168:171], v[92:95]\n\t" \
  "ds_read_b64_tr_b16 v[204:205], v253 offset:24576\n\t" \
  "v_mfma_f32_16x16x32_bf16 v[124:127], v[220:223], v[172:175], v[124:127]\n\t" \
  "ds_read_b64_tr_b16 v[206:207], v253 offset:25600\n\t" \
  "v_mfma_f32_16x16x32_bf16 v[32:35], v[224:227], v[160:163], v[32:35]\n\t" \
  "s_add_u32 m0, s66, 16384\n\t" \
  "s_nop 0\n\t" \
  "global_load_lds_dwordx4 v240, s[60:61]\n\t" \
  "s_add_u32 m0, s66, 17408\n\t" \
  "s_nop 0\n\t" \
  "global_load_lds_dwordx4 v241, s[60:61]\n\t" \
  "v_mfma_f32_16x16x32_bf16 v[64:67], v[224:227], v[164:167], v[64:67]\n\t" \
  "s_add_u32 m0, s66, 18432\n\t" \
  "s_nop 0\n\t" \
  "global_load_lds_dwordx4 v242, s[60:61]\n\t" \
  "v_mfma_f32_16x16x32_bf16 v[96:99], v[224:227], v[168:171], v[96:99]\n\t" \
  "s_add_u32 m0, s66, 19456\n\t" \
  "s_nop 0\n\t" \
  "global_load_lds_dwordx4 v243, s[60:61]\n\t" \
  "v_mfma_f32_16x16x32_bf16 v[128:131], v[224:227], v[172:175], v[128:131]\n\t" \
  "s_add_u32 s60, s60, 128\n\t" \
  "s_addc_u32 s61, s61, 0\n\t" \
  "v_mfma_f32_16x16x32_bf16 v[36:39], v[228:231], v[160:163], v[36:39]\n\t" \
  "s_add_u32 m0, s67, 0\n\t" \
  "s_nop 0\n\t" \
  "global_load_lds_dwordx4 v244, s[62:63]\n\t" \
  "s_add_u32 m0, s67, 1024\n\t" \
  "s_nop 0\n\t" \
  "global_load_lds_dwordx4 v245, s[62:63]\n\t" \
  "v_mfma_f32_16x16x32_bf16 v[68:71], v[228:231], v[164:167], v[68:71]\n\t" \
  "s_add_u32 m0, s67, 8192\n\t" \
  "s_nop 0\n\t" \
  "global_load_lds_dwordx4 v246, s[64:65]\n\t" \
  "v_mfma_f32_16x16x32_bf16 v[100:103], v[228:231], v[168:171], v[100:103]\n\t" \
  "s_add_u32 m0, s67, 9216\n\t" \
  "s_nop 0\n\t" \
  "global_load_lds_dwordx4 v247, s[64:65]\n\t" \
  "v_mfma_f32_16x16x32_bf16 v[132:135], v[228:231], v[172:175], v[132:135]\n\t" \
  "s_add_u32 s62, s62, 110592\n\t" \
  "s_addc_u32 s63, s63, 0\n\t" \
  "s_add_u32 s64, s64, 110592\n\t" \
  "s_addc_u32 s65, s65, 0\n\t" \
  "v_mfma_f32_16x16x32_bf16 v[40:43], v[232:235], v[160:163], v[40:43]\n\t" \
  "v_mfma_f32_16x16x32_bf16 v[72:75], v[232:235], v[164:167], v[72:75]\n\t" \
  "v_mfma_f32_16x16x32_bf16 v[104:107], v[232:235], v[168:171], v[104:107]\n\t" \
  "v_mfma_f32_16x16x32_bf16 v[136:139], v[232:235], v[172:175], v[136:139]\n\t" \
  "v_mfma_f32_16x16x32_bf16 v[44:47], v[236:239], v[160:163], v[44:47]\n\t" \
  "v_mfma_f32_16x16x32_bf16 v[76:79], v[236:239], v[164:167], v[76:79]\n\t" \
  "v_mfma_f32_16x16x32_bf16 v[108:111], v[236:239], v[168:171], v[108:111]\n\t" \
  "v_mfma_f32_16x16x32_bf16 v[140:143], v[236:239], v[172:175], v[140:143]\n\t" \
  "s_waitcnt vmcnt(8) lgkmcnt(0)\n\t" \
  "s_barrier\n\t" \
  "v_mfma_f32_16x16x32_bf16 v[16:19], v[176:179], v[144:147], v[16:19]\n\t" \
  "ds_read_b128 v[160:163], v249 offset:0\n\t" \
  "ds_read_b128 v[164:167], v249 offset:2048\n\t" \
  "v_mfma_f32_16x16x32_bf16 v[48:51], v[176:179], v[148:151], v[48:51]\n\t" \
  "ds_read_b128 v[168:171], v249 offset:4096\n\t" \
  "v_mfma_f32_16x16x32_bf16 v[80:83], v[176:179], v[152:155], v[80:83]\n\t" \
  "ds_read_b128 v[172:175], v249 offset:6144\n\t" \
  "v_mfma_f32_16x16x32_bf16 v[112:115], v[176:179], v[156:159], v[112:115]\n\t" \
  "ds_read_b64_tr_b16 v[208:209], v250 offset:32768\n\t" \
  "v_mfma_f32_16x16x32_bf16 v[20:23], v[180:183], v[144:147], v[20:23]\n\t" \
  "ds_read_b64_tr_b16 v[210:211], v250 offset:33792\n\t" \
  "v_mfma_f32_16x16x32_bf16 v[52:55], v[180:183], v[148:151], v[52:55]\n\t" \
  "ds_read_b64_tr_b16 v[212:213], v250 offset:40960\n\t" \
  "v_mfma_f32_16x16x32_bf16 v[84:87], v[180:183], v[152:155], v[84:87]\n\t" \
  "ds_read_b64_tr_b16 v[214:215], v250 offset:41984\n\t" \
  "v_mfma_f32_16x16x32_bf16 v[116:119], v[180:183], v[156:159], v[116:119]\n\t" \
  "ds_read_b64_tr_b16 v[216:217], v251 offset:32768\n\t" \
  "v_mfma_f32_16x16x32_bf16 v[24:27], v[184:187], v[144:147], v[24:27]\n\t" \
  "ds_read_b64_tr_b16 v[218:219], v251 offset:33792\n\t" \
  "v_mfma_f32_16x16x32_bf16 v[56:59], v[184:187], v[148:151], v[56:59]\n\t" \
  "ds_read_b64_tr_b16 v[220:221], v251 offset:40960\n\t" \
  "v_mfma_f32_16x16x32_bf16 v[88:91], v[184:187], v[152:155], v[88:91]\n\t" \
  "ds_read_b64_tr_b16 v[222:223], v251 offset:41984\n\t" \
  "v_mfma_f32_16x16x32_bf16 v[120:123], v[184:187], v[156:159], v[120:123]\n\t" \
  "ds_read_b64_tr_b16 v[224:225], v252 offset:32768\n\t" \
  "v_mfma_f32_16x16x32_bf16 v[28:31], v[188:191], v[144:147], v[28:31]\n\t" \
  "ds_read_b64_tr_b16 v[226:227], v252 offset:33792\n\t" \
  "v_mfma_f32_16x16x32_bf16 v[60:63], v[188:191], v[148:151], v[60:63]\n\t" \
  "ds_read_b64_tr_b16 v[228:229], v252 offset:40960\n\t" \
  "v_mfma_f32_16x16x32_bf16 v[92:95], v[188:191], v[152:155], v[92:95]\n\t" \
  "ds_read_b64_tr_b16 v[230:231], v252 offset:41984\n\t" \
  "v_mfma_f32_16x16x32_bf16 v[124:127], v[188:191], v[156:159], v[124:127]\n\t" \
  "ds_read_b64_tr_b16 v[232:233], v253 offset:32768\n\t" \
  "v_mfma_f32_16x16x32_bf16 v[32:35], v[192:195], v[144:147], v[32:35]\n\t" \
  "ds_read_b64_tr_b16 v[234:235], v253 offset:33792\n\t" \
  "v_mfma_f32_16x16x32_bf16 v[64:67], v[192:195], v[148:151], v[64:67]\n\t" \
  "ds_read_b64_tr_b16 v[236:237], v253 offset:40960\n\t" \
  "v_mfma_f32_16x16x32_bf16 v[96:99], v[192:195], v[152:155], v[96:99]\n\t" \
  "ds_read_b64_tr_b16 v[238:239], v253 offset:41984\n\t" \
  "v_mfma_f32_16x16x32_bf16 v[128:131], v[192:195], v[156:159], v[128:131]\n\t" \
  "s_add_u32 m0, s67, 16384\n\t" \
  "s_nop 0\n\t" \
  "global_load_lds_dwordx4 v244, s[62:63]\n\t" \
  "v_mfma_f32_16x16x32_bf16 v[36:39], v[196:199], v[144:147], v[36:39]\n\t" \
  "s_add_u32 m0, s67, 17408\n\t" \
  "s_nop 0\n\t" \
  "global_load_lds_dwordx4 v245, s[62:63]\n\t" \
  "v_mfma_f32_16x16x32_bf16 v[68:71], v[196:199], v[148:151], v[68:71]\n\t" \
  "s_add_u32 m0, s67, 24576\n\t" \
  "s_nop 0\n\t" \
  "global_load_lds_dwordx4 v246, s[64:65]\n\t" \
  "v_mfma_f32_16x16x32_bf16 v[100:103], v[196:199], v[152:155], v[100:103]\n\t" \
  "s_add_u32 m0, s67, 25600\n\t" \
  "s_nop 0\n\t" \
  "global_load_lds_dwordx4 v247, s[64:65]\n\t" \
  "v_mfma_f32_16x16x32_bf16 v[132:135], v[196:199], v[156:159], v[132:135]\n\t" \
  "s_add_u32 s62, s62, 110592\n\t" \
  "s_addc_u32 s63, s63, 0\n\t" \
  "s_add_u32 s64, s64, 110592\n\t" \
  "s_addc_u32 s65, s65, 0\n\t" \
  "v_mfma_f32_16x16x32_bf16 v[40:43], v[200:203], v[144:147], v[40:43]\n\t" \
  "v_mfma_f32_16x16x32_bf16 v[72:75], v[200:203], v[148:151], v[72:75]\n\t" \
  "v_mfma_f32_16x16x32_bf16 v[104:107], v[200:203], v[152:155], v[104:107]\n\t" \
  "v_mfma_f32_16x16x32_bf16 v[136:139], v[200:203], v[156:159], v[136:139]\n\t" \
  "v_mfma_f32_16x16x32_bf16 v[44:47], v[204:207], v[144:147], v[44:47]\n\t" \
  "v_mfma_f32_16x16x32_bf16 v[76:79], v[204:207], v[148:151], v[76:79]\n\t" \
  "v_mfma_f32_16x16x32_bf16 v[108:111], v[204:207], v[152:155], v[108:111]\n\t" \
  "v_mfma_f32_16x16x32_bf16 v[140:143], v[204:207], v[156:159], v[140:143]\n\t" \
  "s_waitcnt vmcnt(4) lgkmcnt(0)\n\t" \
  "s_barrier\n\t" \
  "v_mfma_f32_16x16x32_bf16 v[16:19], v[208:211], v[160:163], v[16:19]\n\t" \
  "ds_read_b128 v[144:147], v248 offset:16384\n\t" \
  "v_mfma_f32_16x16x32_bf16 v[48:51], v[208:211], v[164:167], v[48:51]\n\t" \
  "ds_read_b128 v[148:151], v248 offset:18432\n\t" \
  "v_mfma_f32_16x16x32_bf16 v[80:83], v[208:211], v[168:171], v[80:83]\n\t" \
  "ds_read_b128 v[152:155], v248 offset:20480\n\t" \
  "v_mfma_f32_16x16x32_bf16 v[112:115], v[208:211], v[172:175], v[112:115]\n\t" \
  "ds_read_b128 v[156:159], v248 offset:22528\n\t" \
  "v_mfma_f32_16x16x32_bf16 v[20:23], v[212:215], v[160:163], v[20:23]\n\t" \
  "ds_read_b64_tr_b16 v[176:177], v250 offset:0\n\t" \
  "v_mfma_f32_16x16x32_bf16 v[52:55], v[212:215], v[164:167], v[52:55]\n\t" \
  "v_mfma_f32_16x16x32_bf16 v[84:87], v[212:215], v[168:171], v[84:87]\n\t" \
  "ds_read_b64_tr_b16 v[178:179], v250 offset:1024\n\t" \
  "v_mfma_f32_16x16x32_bf16 v[116:119], v[212:215], v[172:175], v[116:119]\n\t" \
  "ds_read_b64_tr_b16 v[180:181], v250 offset:8192\n\t" \
  "v_mfma_f32_16x16x32_bf16 v[24:27], v[216:219], v[160:163], v[24:27]\n\t" \
  "ds_read_b64_tr_b16 v[182:183], v250 offset:9216\n\t" \
  "v_mfma_f32_16x16x32_bf16 v[56:59], v[216:219], v[164:167], v[56:59]\n\t" \
  "ds_read_b64_tr_b16 v[184:185], v251 offset:0\n\t" \
  "v_mfma_f32_16x16x32_bf16 v[88:91], v[216:219], v[168:171], v[88:91]\n\t" \
  "ds_read_b64_tr_b16 v[186:187], v251 offset:1024\n\t" \
  "v_mfma_f32_16x16x32_bf16 v[120:123], v[216:219], v[172:175], v[120:123]\n\t" \
  "v_mfma_f32_16x16x32_bf16 v[28:31], v[220:223], v[160:163], v[28:31]\n\t" \
  "ds_read_b64_tr_b16 v[188:189], v251 offset:8192\n\t" \
  "v_mfma_f32_16x16x32_bf16 v[60:63], v[220:223], v[164:167], v[60:63]\n\t" \
  "ds_read_b64_tr_b16 v[190:191], v251 offset:9216\n\t" \
  "v_mfma_f32_16x16x32_bf16 v[92:95], v[220:223], v[168:171], v[92:95]\n\t" \
  "ds_read_b64_tr_b16 v[192:193], v252 offset:0\n\t" \
  "v_mfma_f32_16x16x32_bf16 v[124:127], v[220:223], v[172:175], v[124:127]\n\t" \
  "ds_read_b64_tr_b16 v[194:195], v252 offset:1024\n\t" \
  "v_mfma_f32_16x16x32_bf16 v[32:35], v[224:227], v[160:163], v[32:35]\n\t" \
  "ds_read_b64_tr_b16 v[196:197], v252 offset:8192\n\t" \
  "v_mfma_f32_16x16x32_bf16 v[64:67], v[224:227], v[164:167], v[64:67]\n\t" \
  "v_mfma_f32_16x16x32_bf16 v[96:99], v[224:227], v[168:171], v[96:99]\n\t" \
  "ds_read_b64_tr_b16 v[198:199], v252 offset:9216\n\t" \
  "v_mfma_f32_16x16x32_bf16 v[128:131], v[224:227], v[172:175], v[128:131]\n\t" \
  "ds_read_b64_tr_b16 v[200:201], v253 offset:0\n\t" \
  "v_mfma_f32_16x16x32_bf16 v[36:39], v[228:231], v[160:163], v[36:39]\n\t" \
  "ds_read_b64_tr_b16 v[202:203], v253 offset:1024\n\t" \
  "v_mfma_f32_16x16x32_bf16 v[68:71], v[228:231], v[164:167], v[68:71]\n\t" \
  "ds_read_b64_tr_b16 v[204:205], v253 offset:8192\n\t" \
  "v_mfma_f32_16x16x32_bf16 v[100:103], v[228:231], v[168:171], v[100:103]\n\t" \
  "ds_read_b64_tr_b16 v[206:207], v253 offset:9216\n\t" \
  "v_mfma_f32_16x16x32_bf16 v[132:135], v[228:231], v[172:175], v[132:135]\n\t" \
  "v_mfma_f32_16x16x32_bf16 v[40:43], v[232:235], v[160:163], v[40:43]\n\t" \
  "v_mfma_f32_16x16x32_bf16 v[72:75], v[232:235], v[164:167], v[72:75]\n\t" \
  "v_mfma_f32_16x16x32_bf16 v[104:107], v[232:235], v[168:171], v[104:107]\n\t" \
  "v_mfma_f32_16x16x32_bf16 v[136:139], v[232:235], v[172:175], v[136:139]\n\t" \
  "v_mfma_f32_16x16x32_bf16 v[44:47], v[236:239], v[160:163], v[44:47]\n\t" \
  "v_mfma_f32_16x16x32_bf16 v[76:79], v[236:239], v[164:167], v[76:79]\n\t" \
  "v_mfma_f32_16x16x32_bf16 v[108:111], v[236:239], v[168:171], v[108:111]\n\t" \
  "v_mfma_f32_16x16x32_bf16 v[140:143], v[236:239], v[172:175], v[140:143]\n\t" \
  "s_waitcnt vmcnt(0) lgkmcnt(0)\n\t" \
  "s_barrier\n\t" \
  "v_mfma_f32_16x16x32_bf16 v[16:19], v[176:179], v[144:147], v[16:19]\n\t" \
  "ds_read_b128 v[160:163], v249 offset:16384\n\t" \
  "v_mfma_f32_16x16x32_bf16 v[48:51], v[176:179], v[148:151], v[48:51]\n\t" \
  "ds_read_b128 v[164:167], v249 offset:18432\n\t" \
  "v_mfma_f32_16x16x32_bf16 v[80:83], v[176:179], v[152:155], v[80:83]\n\t" \
  "ds_read_b128 v[168:171], v249 offset:20480\n\t" \
  "v_mfma_f32_16x16x32_bf16 v[112:115], v[176:179], v[156:159], v[112:115]\n\t" \
  "ds_read_b128 v[172:175], v249 offset:22528\n\t" \
  "v_mfma_f32_16x16x32_bf16 v[20:23], v[180:183], v[144:147], v[20:23]\n\t" \
  "ds_read_b64_tr_b16 v[208:209], v250 offset:16384\n\t" \
  "v_mfma_f32_16x16x32_bf16 v[52:55], v[180:183], v[148:151], v[52:55]\n\t" \
  "v_mfma_f32_16x16x32_bf16 v[84:87], v[180:183], v[152:155], v[84:87]\n\t" \
  "ds_read_b64_tr_b16 v[210:211], v250 offset:17408\n\t" \
  "v_mfma_f32_16x16x32_bf16 v[116:119], v[180:183], v[156:159], v[116:119]\n\t" \
  "ds_read_b64_tr_b16 v[212:213], v250 offset:24576\n\t" \
  "v_mfma_f32_16x16x32_bf16 v[24:27], v[184:187], v[144:147], v[24:27]\n\t" \
  "ds_read_b64_tr_b16 v[214:215], v250 offset:25600\n\t" \
  "v_mfma_f32_16x16x32_bf16 v[56:59], v[184:187], v[148:151], v[56:59]\n\t" \
  "ds_read_b64_tr_b16 v[216:217], v251 offset:16384\n\t" \
  "v_mfma_f32_16x16x32_bf16 v[88:91], v[184:187], v[152:155], v[88:91]\n\t" \
  "ds_read_b64_tr_b16 v[218:219], v251 offset:17408\n\t" \
  "v_mfma_f32_16x16x32_bf16 v[120:123], v[184:187], v[156:159], v[120:123]\n\t" \
  "v_mfma_f32_16x16x32_bf16 v[28:31], v[188:191], v[144:147], v[28:31]\n\t" \
  "ds_read_b64_tr_b16 v[220:221], v251 offset:24576\n\t" \
  "v_mfma_f32_16x16x32_bf16 v[60:63], v[188:191], v[148:151], v[60:63]\n\t" \
  "ds_read_b64_tr_b16 v[222:223], v251 offset:25600\n\t" \
  "v_mfma_f32_16x16x32_bf16 v[92:95], v[188:191], v[152:155], v[92:95]\n\t" \
  "ds_read_b64_tr_b16 v[224:225], v252 offset:16384\n\t" \
  "v_mfma_f32_16x16x32_bf16 v[124:127], v[188:191], v[156:159], v[124:127]\n\t" \
  "ds_read_b64_tr_b16 v[226:227], v252 offset:17408\n\t" \
  "v_mfma_f32_16x16x32_bf16 v[32:35], v[192:195], v[144:147], v[32:35]\n\t" \
  "ds_read_b64_tr_b16 v[228:229], v252 offset:24576\n\t" \
  "v_mfma_f32_16x16x32_bf16 v[64:67], v[192:195], v[148:151], v[64:67]\n\t" \
  "v_mfma_f32_16x16x32_bf16 v[96:99], v[192:195], v[152:155], v[96:99]\n\t" \
  "ds_read_b64_tr_b16 v[230:231], v252 offset:25600\n\t" \
  "v_mfma_f32_16x16x32_bf16 v[128:131], v[192:195], v[156:159], v[128:131]\n\t" \
  "ds_read_b64_tr_b16 v[232:233], v253 offset:16384\n\t" \
  "v_mfma_f32_16x16x32_bf16 v[36:39], v[196:199], v[144:147], v[36:39]\n\t" \
  "ds_read_b64_tr_b16 v[234:235], v253 offset:17408\n\t" \
  "v_mfma_f32_16x16x32_bf16 v[68:71], v[196:199], v[148:151], v[68:71]\n\t" \
  "ds_read_b64_tr_b16 v[236:237], v253 offset:24576\n\t" \
  "v_mfma_f32_16x16x32_bf16 v[100:103], v[196:199], v[152:155], v[100:103]\n\t" \
  "ds_read_b64_tr_b16 v[238:239], v253 offset:25600\n\t" \
  "v_mfma_f32_16x16x32_bf16 v[132:135], v[196:199], v[156:159], v[132:135]\n\t" \
  "v_mfma_f32_16x16x32_bf16 v[40:43], v[200:203], v[144:147], v[40:43]\n\t" \
  "v_mfma_f32_16x16x32_bf16 v[72:75], v[200:203], v[148:151], v[72:75]\n\t" \
  "v_mfma_f32_16x16x32_bf16 v[104:107], v[200:203], v[152:155], v[104:107]\n\t" \
  "v_mfma_f32_16x16x32_bf16 v[136:139], v[200:203], v[156:159], v[136:139]\n\t" \
  "v_mfma_f32_16x16x32_bf16 v[44:47], v[204:207], v[144:147], v[44:47]\n\t" \
  "v_mfma_f32_16x16x32_bf16 v[76:79], v[204:207], v[148:151], v[76:79]\n\t" \
  "v_mfma_f32_16x16x32_bf16 v[108:111], v[204:207], v[152:155], v[108:111]\n\t" \
  "v_mfma_f32_16x16x32_bf16 v[140:143], v[204:207], v[156:159], v[140:143]\n\t" \
  "s_waitcnt lgkmcnt(0)\n\t" \
  "v_mfma_f32_16x16x32_bf16 v[16:19], v[208:211], v[160:163], v[16:19]\n\t" \
  "v_mfma_f32_16x16x32_bf16 v[48:51], v[208:211], v[164:167], v[48:51]\n\t" \
  "v_mfma_f32_16x16x32_bf16 v[80:83], v[208:211], v[168:171], v[80:83]\n\t" \
  "v_mfma_f32_16x16x32_bf16 v[112:115], v[208:211], v[172:175], v[112:115]\n\t" \
  "v_mfma_f32_16x16x32_bf16 v[20:23], v[212:215], v[160:163], v[20:23]\n\t" \
  "v_mfma_f32_16x16x32_bf16 v[52:55], v[212:215], v[164:167], v[52:55]\n\t" \
  "v_mfma_f32_16x16x32_bf16 v[84:87], v[212:215], v[168:171], v[84:87]\n\t" \
  "v_mfma_f32_16x16x32_bf16 v[116:119], v[212:215], v[172:175], v[116:119]\n\t" \
  "v_mfma_f32_16x16x32_bf16 v[24:27], v[216:219], v[160:163], v[24:27]\n\t" \
  "v_mfma_f32_16x16x32_bf16 v[56:59], v[216:219], v[164:167], v[56:59]\n\t" \
  "v_mfma_f32_16x16x32_bf16 v[88:91], v[216:219], v[168:171], v[88:91]\n\t" \
  "v_mfma_f32_16x16x32_bf16 v[120:123], v[216:219], v[172:175], v[120:123]\n\t" \
  "v_mfma_f32_16x16x32_bf16 v[28:31], v[220:223], v[160:163], v[28:31]\n\t" \
  "v_mfma_f32_16x16x32_bf16 v[60:63], v[220:223], v[164:167], v[60:63]\n\t" \
  "v_mfma_f32_16x16x32_bf16 v[92:95], v[220:223], v[168:171], v[92:95]\n\t" \
  "v_mfma_f32_16x16x32_bf16 v[124:127], v[220:223], v[172:175], v[124:127]\n\t" \
  "v_mfma_f32_16x16x32_bf16 v[32:35], v[224:227], v[160:163], v[32:35]\n\t" \
  "v_mfma_f32_16x16x32_bf16 v[64:67], v[224:227], v[164:167], v[64:67]\n\t" \
  "v_mfma_f32_16x16x32_bf16 v[96:99], v[224:227], v[168:171], v[96:99]\n\t" \
  "v_mfma_f32_16x16x32_bf16 v[128:131], v[224:227], v[172:175], v[128:131]\n\t" \
  "v_mfma_f32_16x16x32_bf16 v[36:39], v[228:231], v[160:163], v[36:39]\n\t" \
  "v_mfma_f32_16x16x32_bf16 v[68:71], v[228:231], v[164:167], v[68:71]\n\t" \
  "v_mfma_f32_16x16x32_bf16 v[100:103], v[228:231], v[168:171], v[100:103]\n\t" \
  "v_mfma_f32_16x16x32_bf16 v[132:135], v[228:231], v[172:175], v[132:135]\n\t" \
  "v_mfma_f32_16x16x32_bf16 v[40:43], v[232:235], v[160:163], v[40:43]\n\t" \
  "v_mfma_f32_16x16x32_bf16 v[72:75], v[232:235], v[164:167], v[72:75]\n\t" \
  "v_mfma_f32_16x16x32_bf16 v[104:107], v[232:235], v[168:171], v[104:107]\n\t" \
  "v_mfma_f32_16x16x32_bf16 v[136:139], v[232:235], v[172:175], v[136:139]\n\t" \
  "v_mfma_f32_16x16x32_bf16 v[44:47], v[236:239], v[160:163], v[44:47]\n\t" \
  "v_mfma_f32_16x16x32_bf16 v[76:79], v[236:239], v[164:167], v[76:79]\n\t" \
  "v_mfma_f32_16x16x32_bf16 v[108:111], v[236:239], v[168:171], v[108:111]\n\t" \
  "v_mfma_f32_16x16x32_bf16 v[140:143], v[236:239], v[172:175], v[140:143]\n\t" \
  "s_nop 15\n\t" \
  ""
#define UP_CLOB "v144","v145","v146","v147","v148","v149",V10(15),V10(16),V10(17),V10(18),V10(19),V10(20),V10(21),V10(22),V10(23),"scc","memory"
DI void ffn_up(const uint16_t* x, const uint16_t* wg, const uint16_t* wu, char* ws, int pp, int cg, char* smem) {
    const int lane = threadIdx.x & 63, w = rfl(threadIdx.x >> 6), th = w & 1, j0 = (w >> 1) * 4;
    const unsigned lds0 = (unsigned)(uintptr_t)smem;
    unsigned vdx[4], vdw[4], vx[2], vw[4];
#pragma unroll
    for (int i = 0; i < 4; ++i) { const int row = (4 * w + i) * 8 + (lane >> 3); vdx[i] = row * 1280 + (((lane & 7) ^ ((row >> 1) & 7)) << 4); }
#pragma unroll
    for (int i = 0; i < 4; ++i) { const int k = (2 * w + (i & 1)) * 4 + (lane >> 4); int c = (lane & 15) ^ ((k & 3) << 1) ^ (((k >> 3) & 1) << 3); if (cg == 13) c &= 7; vdw[i] = k * 3456 + c * 16; }
    const int t16 = lane & 15, kq = lane >> 4, q = t16 >> 2, p = lane & 3;
    const int xrow = th * 64 + t16;
#pragma unroll
    for (int s2 = 0; s2 < 2; ++s2) vx[s2] = lds0 + xrow * 128 + (((4 * s2 + kq) ^ ((xrow >> 1) & 7)) << 4);
#pragma unroll
    for (int jj = 0; jj < 4; ++jj) vw[jj] = lds0 + 32768 + (8 * kq + q) * 256 + (((2 * (j0 + jj) + (p >> 1)) ^ (q << 1) ^ ((kq & 1) << 3)) << 4) + (p & 1) * 8;
    f32x16 acc[8];
#pragma unroll
    for (int a = 0; a < 8; ++a) acc[a] = f32x16{};
    uint64_t xp = uni64(x + (size_t)pp * 128 * D), gp = uni64(wg + cg * 128), up_ = uni64(wu + cg * 128);
    asm volatile(UP3_ASM
        : "+{v[16:31]}"(acc[0]), "+{v[32:47]}"(acc[1]), "+{v[48:63]}"(acc[2]), "+{v[64:79]}"(acc[3]), "+{v[80:95]}"(acc[4]), "+{v[96:111]}"(acc[5]), "+{v[112:127]}"(acc[6]), "+{v[128:143]}"(acc[7]),
          "+{s[60:61]}"(xp), "+{s[62:63]}"(gp), "+{s[64:65]}"(up_)
        : "{v240}"(vdx[0]), "{v241}"(vdx[1]), "{v242}"(vdx[2]), "{v243}"(vdx[3]), "{v244}"(vdw[0]), "{v245}"(vdw[1]), "{v246}"(vdw[2]), "{v247}"(vdw[3]),
          "{v248}"(vx[0]), "{v249}"(vx[1]), "{v250}"(vw[0]), "{v251}"(vw[1]), "{v252}"(vw[2]), "{v253}"(vw[3]),
          "{s66}"(rfl(lds0 + 4 * w * 1024)), "{s67}"(rfl(lds0 + 32768 + 2 * w * 1024)), [p2] "n"(GP_UP2)
        : UP_CLOB);
    PRIO(GP_HI);
    const rsrc_t R = ws_rsrc(ws);
    const int ln = fresh_lane();
    if (cg * 8 + j0 < 108) {
#pragma unroll
        for (int tb = 0; tb < 4; ++tb) {
            const unsigned hb = WS_H + ((pp * 8 + th * 4 + tb) * 54 + cg * 4 + (j0 >> 1)) * 1024 + ln * 16;
#pragma unroll
            for (int pr = 0; pr < 2; ++pr) { const f32x16& t = acc[2 * tb + pr];
                const f32x2 h0 = geglu2(f32x2{t[0], t[1]}, f32x2{t[4], t[5]}), h1 = geglu2(f32x2{t[2], t[3]}, f32x2{t[6], t[7]});
                const f32x2 h2 = geglu2(f32x2{t[8], t[9]}, f32x2{t[12], t[13]}), h3 = geglu2(f32x2{t[10], t[11]}, f32x2{t[14], t[15]});
                st16(R, hb + pr * 1024, u32x4{cvtpk(h0[0], h0[1]), cvtpk(h1[0], h1[1]), cvtpk(h2[0], h2[1]), cvtpk(h3[0], h3[1])});
            }
        }
    }
}

DI void retile(const uint16_t* wd, const uint16_t* wo, char* ws, int rt, char* smem) {
    const int lane = threadIdx.x & 63, w = rfl(threadIdx.x >> 6), kq = lane >> 4, q = (lane & 15) >> 2, p = lane & 3;
    const unsigned lds0 = (unsigned)(uintptr_t)smem; lds_ptr L = (lds_ptr)smem;
    const rsrc_t R = ws_rsrc(ws);
    auto issue = [&](int st, int slot) {
#pragma unroll
        for (int gg = 0; gg < 2; ++gg) { const int gi = 2 * w + gg; int row0;
            if (st < 54) row0 = st * 32 + gi * 4;
            else { const int pi = 4 * (st - 54) + (gi & 3), second = gi >> 2, hgp = pi / 10, pp = pi % 10;
                row0 = pp < 8 ? hgp * 80 + (pp >> 1) * 20 + 4 * (pp & 1) + 8 * second : hgp * 80 + (2 * (pp - 8) + second) * 20 + 16; }
            const char* src = (const char*)(st < 54 ? wd : wo) + (size_t)row0 * 1280;
#pragma unroll
            for (int i = 0; i < 5; ++i) glds_s(src + i * 1024, lane * 16, lds0 + slot * 40960 + (gi * 5 + i) * 1024); }
    };
    int st = rt, slot = 0;
    issue(st, 0);
    for (; st < 74; st += 32) {
        if (st + 32 < 74) { issue(st + 32, slot ^ 1); WAITBAR(10); } else WAITBAR(0);
#pragma unroll
        for (int i = 0; i < 2; ++i) { const int ng = 2 * w + i;
            lds_ptr a = L + slot * 40960 + (4 * kq + q) * 1280 + (ng * 80 + 4 * p) * 2;
#pragma unroll
            for (int c = 0; c < 5; ++c) st16(R, WS_WT + ((ng * 74 + st) * 5 + c) * 1024 + lane * 16, __builtin_bit_cast(u32x4, tr2(a + 32 * c, 16 * 1280)));
        }
        LBAR();
        slot ^= 1;
    }
}

#define DOWN3_ASM \
  "s_setprio %[pl]\n\t" \
  "global_load_dwordx4 v[96:99], v240, s[60:61] sc1\n\t" \
  "global_load_dwordx4 v[100:103], v240, s[60:61] offset:1024 sc1\n\t" \
  "global_load_dwordx4 v[104:107], v240, s[60:61] offset:2048 sc1\n\t" \
  "global_load_dwordx4 v[108:111], v240, s[60:61] offset:3072 sc1\n\t" \
  "global_load_dwordx4 v[112:115], v245, s[60:61] sc1\n\t" \
  "global_load_dwordx4 v[116:119], v241, s[62:63] sc1\n\t" \
  "global_load_dwordx4 v[120:123], v242, s[62:63] sc1\n\t" \
  "global_load_dwordx4 v[124:127], v243, s[62:63] sc1\n\t" \
  "global_load_dwordx4 v[128:131], v244, s[62:63] sc1\n\t" \
  "s_add_u32 s60, s60, 5120\n\t" \
  "s_addc_u32 s61, s61, 0\n\t" \
  "s_add_u32 s62, s62, 1024\n\t" \
  "s_addc_u32 s63, s63, 0\n\t" \
  "global_load_dwordx4 v[132:135], v240, s[60:61] sc1\n\t" \
  "global_load_dwordx4 v[136:139], v240, s[60:61] offset:1024 sc1\n\t" \
  "global_load_dwordx4 v[140:143], v240, s[60:61] offset:2048 sc1\n\t" \
  "global_load_dwordx4 v[144:147], v240, s[60:61] offset:3072 sc1\n\t" \
  "global_load_dwordx4 v[148:151], v245, s[60:61] sc1\n\t" \
  "global_load_dwordx4 v[152:155], v241, s[62:63] sc1\n\t" \
  "global_load_dwordx4 v[156:159], v242, s[62:63] sc1\n\t" \
  "global_load_dwordx4 v[160:163], v243, s[62:63] sc1\n\t" \
  "global_load_dwordx4 v[164:167], v244, s[62:63] sc1\n\t" \
  "s_add_u32 s60, s60, 5120\n\t" \
  "s_addc_u32 s61, s61, 0\n\t" \
  "s_add_u32 s62, s62, 1024\n\t" \
  "s_addc_u32 s63, s63, 0\n\t" \
  "global_load_dwordx4 v[168:171], v240, s[60:61] sc1\n\t" \
  "global_load_dwordx4 v[172:175], v240, s[60:61] offset:1024 sc1\n\t" \
  "global_load_dwordx4 v[176:179], v240, s[60:61] offset:2048 sc1\n\t" \
  "global_load_dwordx4 v[180:183], v240, s[60:61] offset:3072 sc1\n\t" \
  "global_load_dwordx4 v[184:187], v245, s[60:61] sc1\n\t" \
  "global_load_dwordx4 v[188:191], v241, s[62:63] sc1\n\t" \
  "global_load_dwordx4 v[192:195], v242, s[62:63] sc1\n\t" \
  "global_load_dwordx4 v[196:199], v243, s[62:63] sc1\n\t" \
  "global_load_dwordx4 v[200:203], v244, s[62:63] sc1\n\t" \
  "s_add_u32 s60, s60, 5120\n\t" \
  "s_addc_u32 s61, s61, 0\n\t" \
  "s_add_u32 s62, s62, 1024\n\t" \
  "s_addc_u32 s63, s63, 0\n\t" \
  "global_load_dwordx4 v[204:207], v240, s[60:61] sc1\n\t" \
  "global_load_dwordx4 v[208:211], v240, s[60:61] offset:1024 sc1\n\t" \
  "global_load_dwordx4 v[212:215], v240, s[60:61] offset:2048 sc1\n\t" \
  "global_load_dwordx4 v[216:219], v240, s[60:61] offset:3072 sc1\n\t" \
  "global_load_dwordx4 v[220:223], v245, s[60:61] sc1\n\t" \
  "global_load_dwordx4 v[224:227], v241, s[62:63] sc1\n\t" \
  "global_load_dwordx4 v[228:231], v242, s[62:63] sc1\n\t" \
  "global_load_dwordx4 v[232:235], v243, s[62:63] sc1\n\t" \
  "global_load_dwordx4 v[236:239], v244, s[62:63] sc1\n\t" \
  "s_add_u32 s60, s60, 5120\n\t" \
  "s_addc_u32 s61, s61, 0\n\t" \
  "s_add_u32 s62, s62, 1024\n\t" \
  "s_addc_u32 s63, s63, 0\n\t" \
  "s_waitcnt vmcnt(27)\n\t" \
  "v_mfma_f32_16x16x32_bf16 v[16:19], v[96:99], v[116:119], v[16:19]\n\t" \
  "v_mfma_f32_16x16x32_bf16 v[36:39], v[96:99], v[120:123], v[36:39]\n\t" \
  "v_mfma_f32_16x16x32_bf16 v[56:59], v[96:99], v[124:127], v[56:59]\n\t" \
  "v_mfma_f32_16x16x32_bf16 v[76:79], v[96:99], v[128:131], v[76:79]\n\t" \
  "v_mfma_f32_16x16x32_bf16 v[20:23], v[100:103], v[116:119], v[20:23]\n\t" \
  "v_mfma_f32_16x16x32_bf16 v[40:43], v[100:103], v[120:123], v[40:43]\n\t" \
  "v_mfma_f32_16x16x32_bf16 v[60:63], v[100:103], v[124:127], v[60:63]\n\t" \
  "v_mfma_f32_16x16x32_bf16 v[80:83], v[100:103], v[128:131], v[80:83]\n\t" \
  "v_mfma_f32_16x16x32_bf16 v[24:27], v[104:107], v[116:119], v[24:27]\n\t" \
  "v_mfma_f32_16x16x32_bf16 v[44:47], v[104:107], v[120:123], v[44:47]\n\t" \
  "v_mfma_f32_16x16x32_bf16 v[64:67], v[104:107], v[124:127], v[64:67]\n\t" \
  "v_mfma_f32_16x16x32_bf16 v[84:87], v[104:107], v[128:131], v[84:87]\n\t" \
  "v_mfma_f32_16x16x32_bf16 v[28:31], v[108:111], v[116:119], v[28:31]\n\t" \
  "v_mfma_f32_16x16x32_bf16 v[48:51], v[108:111], v[120:123], v[48:51]\n\t" \
  "v_mfma_f32_16x16x32_bf16 v[68:71], v[108:111], v[124:127], v[68:71]\n\t" \
  "v_mfma_f32_16x16x32_bf16 v[88:91], v[108:111], v[128:131], v[88:91]\n\t" \
  "v_mfma_f32_16x16x32_bf16 v[32:35], v[112:115], v[116:119], v[32:35]\n\t" \
  "v_mfma_f32_16x16x32_bf16 v[52:55], v[112:115], v[120:123], v[52:55]\n\t" \
  "v_mfma_f32_16x16x32_bf16 v[72:75], v[112:115], v[124:127], v[72:75]\n\t" \
  "v_mfma_f32_16x16x32_bf16 v[92:95], v[112:115], v[128:131], v[92:95]\n\t" \
  "global_load_dwordx4 v[96:99], v240, s[60:61] sc1\n\t" \
  "global_load_dwordx4 v[100:103], v240, s[60:61] offset:1024 sc1\n\t" \
  "global_load_dwordx4 v[104:107], v240, s[60:61] offset:2048 sc1\n\t" \
  "global_load_dwordx4 v[108:111], v240, s[60:61] offset:3072 sc1\n\t" \
  "global_load_dwordx4 v[112:115], v245, s[60:61] sc1\n\t" \
  "global_load_dwordx4 v[116:119], v241, s[62:63] sc1\n\t" \
  "global_load_dwordx4 v[120:123], v242, s[62:63] sc1\n\t" \
  "global_load_dwordx4 v[124:127], v243, s[62:63] sc1\n\t" \
  "global_load_dwordx4 v[128:131], v244, s[62:63] sc1\n\t" \
  "s_add_u32 s60, s60, 5120\n\t" \
  "s_addc_u32 s61, s61, 0\n\t" \
  "s_add_u32 s62, s62, 1024\n\t" \
  "s_addc_u32 s63, s63, 0\n\t" \
  "s_waitcnt vmcnt(27)\n\t" \
  "v_mfma_f32_16x16x32_bf16 v[16:19], v[132:135], v[152:155], v[16:19]\n\t" \
  "v_mfma_f32_16x16x32_bf16 v[36:39], v[132:135], v[156:159], v[36:39]\n\t" \
  "v_mfma_f32_16x16x32_bf16 v[56:59], v[132:135], v[160:163], v[56:59]\n\t" \
  "v_mfma_f32_16x16x32_bf16 v[76:79], v[132:135], v[164:167], v[76:79]\n\t" \
  "v_mfma_f32_16x16x32_bf16 v[20:23], v[136:139], v[152:155], v[20:23]\n\t" \
  "v_mfma_f32_16x16x32_bf16 v[40:43], v[136:139], v[156:159], v[40:43]\n\t" \
  "v_mfma_f32_16x16x32_bf16 v[60:63], v[136:139], v[160:163], v[60:63]\n\t" \
  "v_mfma_f32_16x16x32_bf16 v[80:83], v[136:139], v[164:167], v[80:83]\n\t" \
  "v_mfma_f32_16x16x32_bf16 v[24:27], v[140:143], v[152:155], v[24:27]\n\t" \
  "v_mfma_f32_16x16x32_bf16 v[44:47], v[140:143], v[156:159], v[44:47]\n\t" \
  "v_mfma_f32_16x16x32_bf16 v[64:67], v[140:143], v[160:163], v[64:67]\n\t" \
  "v_mfma_f32_16x16x32_bf16 v[84:87], v[140:143], v[164:167], v[84:87]\n\t" \
  "v_mfma_f32_16x16x32_bf16 v[28:31], v[144:147], v[152:155], v[28:31]\n\t" \
  "v_mfma_f32_16x16x32_bf16 v[48:51], v[144:147], v[156:159], v[48:51]\n\t" \
  "v_mfma_f32_16x16x32_bf16 v[68:71], v[144:147], v[160:163], v[68:71]\n\t" \
  "v_mfma_f32_16x16x32_bf16 v[88:91], v[144:147], v[164:167], v[88:91]\n\t" \
  "v_mfma_f32_16x16x32_bf16 v[32:35], v[148:151], v[152:155], v[32:35]\n\t" \
  "v_mfma_f32_16x16x32_bf16 v[52:55], v[148:151], v[156:159], v[52:55]\n\t" \
  "v_mfma_f32_16x16x32_bf16 v[72:75], v[148:151], v[160:163], v[72:75]\n\t" \
  "v_mfma_f32_16x16x32_bf16 v[92:95], v[148:151], v[164:167], v[92:95]\n\t" \
  "global_load_dwordx4 v[132:135], v240, s[60:61] sc1\n\t" \
  "global_load_dwordx4 v[136:139], v240, s[60:61] offset:1024 sc1\n\t" \
  "global_load_dwordx4 v[140:143], v240, s[60:61] offset:2048 sc1\n\t" \
  "global_load_dwordx4 v[144:147], v240, s[60:61] offset:3072 sc1\n\t" \
  "global_load_dwordx4 v[148:151], v245, s[60:61] sc1\n\t" \
  "global_load_dwordx4 v[152:155], v241, s[62:63] sc1\n\t" \
  "global_load_dwordx4 v[156:159], v242, s[62:63] sc1\n\t" \
  "global_load_dwordx4 v[160:163], v243, s[62:63] sc1\n\t" \
  "global_load_dwordx4 v[164:167], v244, s[62:63] sc1\n\t" \
  "s_add_u32 s60, s60, 5120\n\t" \
  "s_addc_u32 s61, s61, 0\n\t" \
  "s_add_u32 s62, s62, 1024\n\t" \
  "s_addc_u32 s63, s63, 0\n\t" \
  "s_waitcnt vmcnt(27)\n\t" \
  "v_mfma_f32_16x16x32_bf16 v[16:19], v[168:171], v[188:191], v[16:19]\n\t" \
  "v_mfma_f32_16x16x32_bf16 v[36:39], v[168:171], v[192:195], v[36:39]\n\t" \
  "v_mfma_f32_16x16x32_bf16 v[56:59], v[168:171], v[196:199], v[56:59]\n\t" \
  "v_mfma_f32_16x16x32_bf16 v[76:79], v[168:171], v[200:203], v[76:79]\n\t" \
  "v_mfma_f32_16x16x32_bf16 v[20:23], v[172:175], v[188:191], v[20:23]\n\t" \
  "v_mfma_f32_16x16x32_bf16 v[40:43], v[172:175], v[192:195], v[40:43]\n\t" \
  "v_mfma_f32_16x16x32_bf16 v[60:63], v[172:175], v[196:199], v[60:63]\n\t" \
  "v_mfma_f32_16x16x32_bf16 v[80:83], v[172:175], v[200:203], v[80:83]\n\t" \
  "v_mfma_f32_16x16x32_bf16 v[24:27], v[176:179], v[188:191], v[24:27]\n\t" \
  "v_mfma_f32_16x16x32_bf16 v[44:47], v[176:179], v[192:195], v[44:47]\n\t" \
  "v_mfma_f32_16x16x32_bf16 v[64:67], v[176:179], v[196:199], v[64:67]\n\t" \
  "v_mfma_f32_16x16x32_bf16 v[84:87], v[176:179], v[200:203], v[84:87]\n\t" \
  "v_mfma_f32_16x16x32_bf16 v[28:31], v[180:183], v[188:191], v[28:31]\n\t" \
  "v_mfma_f32_16x16x32_bf16 v[48:51], v[180:183], v[192:195], v[48:51]\n\t" \
  "v_mfma_f32_16x16x32_bf16 v[68:71], v[180:183], v[196:199], v[68:71]\n\t" \
  "v_mfma_f32_16x16x32_bf16 v[88:91], v[180:183], v[200:203], v[88:91]\n\t" \
  "v_mfma_f32_16x16x32_bf16 v[32:35], v[184:187], v[188:191], v[32:35]\n\t" \
  "v_mfma_f32_16x16x32_bf16 v[52:55], v[184:187], v[192:195], v[52:55]\n\t" \
  "v_mfma_f32_16x16x32_bf16 v[72:75], v[184:187], v[196:199], v[72:75]\n\t" \
  "v_mfma_f32_16x16x32_bf16 v[92:95], v[184:187], v[200:203], v[92:95]\n\t" \
  "global_load_dwordx4 v[168:171], v240, s[60:61] sc1\n\t" \
  "global_load_dwordx4 v[172:175], v240, s[60:61] offset:1024 sc1\n\t" \
  "global_load_dwordx4 v[176:179], v240, s[60:61] offset:2048 sc1\n\t" \
  "global_load_dwordx4 v[180:183], v240, s[60:61] offset:3072 sc1\n\t" \
  "global_load_dwordx4 v[184:187], v245, s[60:61] sc1\n\t" \
  "global_load_dwordx4 v[188:191], v241, s[62:63] sc1\n\t" \
  "global_load_dwordx4 v[192:195], v242, s[62:63] sc1\n\t" \
  "global_load_dwordx4 v[196:199], v243, s[62:63] sc1\n\t" \
  "global_load_dwordx4 v[200:203], v244, s[62:63] sc1\n\t" \
  "s_add_u32 s60, s60, 5120\n\t" \
  "s_addc_u32 s61, s61, 0\n\t" \
  "s_add_u32 s62, s62, 1024\n\t" \
  "s_addc_u32 s63, s63, 0\n\t" \
  "s_waitcnt vmcnt(27)\n\t" \
  "v_mfma_f32_16x16x32_bf16 v[16:19], v[204:207], v[224:227], v[16:19]\n\t" \
  "v_mfma_f32_16x16x32_bf16 v[36:39], v[204:207], v[228:231], v[36:39]\n\t" \
  "v_mfma_f32_16x16x32_bf16 v[56:59], v[204:207], v[232:235], v[56:59]\n\t" \
  "v_mfma_f32_16x16x32_bf16 v[76:79], v[204:207], v[236:239], v[76:79]\n\t" \
  "v_mfma_f32_16x16x32_bf16 v[20:23], v[208:211], v[224:227], v[20:23]\n\t" \
  "v_mfma_f32_16x16x32_bf16 v[40:43], v[208:211], v[228:231], v[40:43]\n\t" \
  "v_mfma_f32_16x16x32_bf16 v[60:63], v[208:211], v[232:235], v[60:63]\n\t" \
  "v_mfma_f32_16x16x32_bf16 v[80:83], v[208:211], v[236:239], v[80:83]\n\t" \
  "v_mfma_f32_16x16x32_bf16 v[24:27], v[212:215], v[224:227], v[24:27]\n\t" \
  "v_mfma_f32_16x16x32_bf16 v[44:47], v[212:215], v[228:231], v[44:47]\n\t" \
  "v_mfma_f32_16x16x32_bf16 v[64:67], v[212:215], v[232:235], v[64:67]\n\t" \
  "v_mfma_f32_16x16x32_bf16 v[84:87], v[212:215], v[236:239], v[84:87]\n\t" \
  "v_mfma_f32_16x16x32_bf16 v[28:31], v[216:219], v[224:227], v[28:31]\n\t" \
  "v_mfma_f32_16x16x32_bf16 v[48:51], v[216:219], v[228:231], v[48:51]\n\t" \
  "v_mfma_f32_16x16x32_bf16 v[68:71], v[216:219], v[232:235], v[68:71]\n\t" \
  "v_mfma_f32_16x16x32_bf16 v[88:91], v[216:219], v[236:239], v[88:91]\n\t" \
  "v_mfma_f32_16x16x32_bf16 v[32:35], v[220:223], v[224:227], v[32:35]\n\t" \
  "v_mfma_f32_16x16x32_bf16 v[52:55], v[220:223], v[228:231], v[52:55]\n\t" \
  "v_mfma_f32_16x16x32_bf16 v[72:75], v[220:223], v[232:235], v[72:75]\n\t" \
  "v_mfma_f32_16x16x32_bf16 v[92:95], v[220:223], v[236:239], v[92:95]\n\t" \
  "global_load_dwordx4 v[204:207], v240, s[60:61] sc1\n\t" \
  "global_load_dwordx4 v[208:211], v240, s[60:61] offset:1024 sc1\n\t" \
  "global_load_dwordx4 v[212:215], v240, s[60:61] offset:2048 sc1\n\t" \
  "global_load_dwordx4 v[216:219], v240, s[60:61] offset:3072 sc1\n\t" \
  "global_load_dwordx4 v[220:223], v245, s[60:61] sc1\n\t" \
  "global_load_dwordx4 v[224:227], v241, s[62:63] sc1\n\t" \
  "global_load_dwordx4 v[228:231], v242, s[62:63] sc1\n\t" \
  "global_load_dwordx4 v[232:235], v243, s[62:63] sc1\n\t" \
  "global_load_dwordx4 v[236:239], v244, s[62:63] sc1\n\t" \
  "s_add_u32 s60, s60, 5120\n\t" \
  "s_addc_u32 s61, s61, 0\n\t" \
  "s_add_u32 s62, s62, 1024\n\t" \
  "s_addc_u32 s63, s63, 0\n\t" \
  "s_waitcnt vmcnt(27)\n\t" \
  "v_mfma_f32_16x16x32_bf16 v[16:19], v[96:99], v[116:119], v[16:19]\n\t" \
  "v_mfma_f32_16x16x32_bf16 v[36:39], v[96:99], v[120:123], v[36:39]\n\t" \
  "v_mfma_f32_16x16x32_bf16 v[56:59], v[96:99], v[124:127], v[56:59]\n\t" \
  "v_mfma_f32_16x16x32_bf16 v[76:79], v[96:99], v[128:131], v[76:79]\n\t" \
  "v_mfma_f32_16x16x32_bf16 v[20:23], v[100:103], v[116:119], v[20:23]\n\t" \
  "v_mfma_f32_16x16x32_bf16 v[40:43], v[100:103], v[120:123], v[40:43]\n\t" \
  "v_mfma_f32_16x16x32_bf16 v[60:63], v[100:103], v[124:127], v[60:63]\n\t" \
  "v_mfma_f32_16x16x32_bf16 v[80:83], v[100:103], v[128:131], v[80:83]\n\t" \
  "v_mfma_f32_16x16x32_bf16 v[24:27], v[104:107], v[116:119], v[24:27]\n\t" \
  "v_mfma_f32_16x16x32_bf16 v[44:47], v[104:107], v[120:123], v[44:47]\n\t" \
  "v_mfma_f32_16x16x32_bf16 v[64:67], v[104:107], v[124:127], v[64:67]\n\t" \
  "v_mfma_f32_16x16x32_bf16 v[84:87], v[104:107], v[128:131], v[84:87]\n\t" \
  "v_mfma_f32_16x16x32_bf16 v[28:31], v[108:111], v[116:119], v[28:31]\n\t" \
  "v_mfma_f32_16x16x32_bf16 v[48:51], v[108:111], v[120:123], v[48:51]\n\t" \
  "v_mfma_f32_16x16x32_bf16 v[68:71], v[108:111], v[124:127], v[68:71]\n\t" \
  "v_mfma_f32_16x16x32_bf16 v[88:91], v[108:111], v[128:131], v[88:91]\n\t" \
  "v_mfma_f32_16x16x32_bf16 v[32:35], v[112:115], v[116:119], v[32:35]\n\t" \
  "v_mfma_f32_16x16x32_bf16 v[52:55], v[112:115], v[120:123], v[52:55]\n\t" \
  "v_mfma_f32_16x16x32_bf16 v[72:75], v[112:115], v[124:127], v[72:75]\n\t" \
  "v_mfma_f32_16x16x32_bf16 v[92:95], v[112:115], v[128:131], v[92:95]\n\t" \
  "global_load_dwordx4 v[96:99], v240, s[60:61] sc1\n\t" \
  "global_load_dwordx4 v[100:103], v240, s[60:61] offset:1024 sc1\n\t" \
  "global_load_dwordx4 v[104:107], v240, s[60:61] offset:2048 sc1\n\t" \
  "global_load_dwordx4 v[108:111], v240, s[60:61] offset:3072 sc1\n\t" \
  "global_load_dwordx4 v[112:115], v245, s[60:61] sc1\n\t" \
  "global_load_dwordx4 v[116:119], v241, s[62:63] sc1\n\t" \
  "global_load_dwordx4 v[120:123], v242, s[62:63] sc1\n\t" \
  "global_load_dwordx4 v[124:127], v243, s[62:63] sc1\n\t" \
  "global_load_dwordx4 v[128:131], v244, s[62:63] sc1\n\t" \
  "s_add_u32 s60, s60, 5120\n\t" \
  "s_addc_u32 s61, s61, 0\n\t" \
  "s_add_u32 s62, s62, 1024\n\t" \
  "s_addc_u32 s63, s63, 0\n\t" \
  "s_waitcnt vmcnt(27)\n\t" \
  "v_mfma_f32_16x16x32_bf16 v[16:19], v[132:135], v[152:155], v[16:19]\n\t" \
  "v_mfma_f32_16x16x32_bf16 v[36:39], v[132:135], v[156:159], v[36:39]\n\t" \
  "v_mfma_f32_16x16x32_bf16 v[56:59], v[132:135], v[160:163], v[56:59]\n\t" \
  "v_mfma_f32_16x16x32_bf16 v[76:79], v[132:135], v[164:167], v[76:79]\n\t" \
  "v_mfma_f32_16x16x32_bf16 v[20:23], v[136:139], v[152:155], v[20:23]\n\t" \
  "v_mfma_f32_16x16x32_bf16 v[40:43], v[136:139], v[156:159], v[40:43]\n\t" \
  "v_mfma_f32_16x16x32_bf16 v[60:63], v[136:139], v[160:163], v[60:63]\n\t" \
  "v_mfma_f32_16x16x32_bf16 v[80:83], v[136:139], v[164:167], v[80:83]\n\t" \
  "v_mfma_f32_16x16x32_bf16 v[24:27], v[140:143], v[152:155], v[24:27]\n\t" \
  "v_mfma_f32_16x16x32_bf16 v[44:47], v[140:143], v[156:159], v[44:47]\n\t" \
  "v_mfma_f32_16x16x32_bf16 v[64:67], v[140:143], v[160:163], v[64:67]\n\t" \
  "v_mfma_f32_16x16x32_bf16 v[84:87], v[140:143], v[164:167], v[84:87]\n\t" \
  "v_mfma_f32_16x16x32_bf16 v[28:31], v[144:147], v[152:155], v[28:31]\n\t" \
  "v_mfma_f32_16x16x32_bf16 v[48:51], v[144:147], v[156:159], v[48:51]\n\t" \
  "v_mfma_f32_16x16x32_bf16 v[68:71], v[144:147], v[160:163], v[68:71]\n\t" \
  "v_mfma_f32_16x16x32_bf16 v[88:91], v[144:147], v[164:167], v[88:91]\n\t" \
  "v_mfma_f32_16x16x32_bf16 v[32:35], v[148:151], v[152:155], v[32:35]\n\t" \
  "v_mfma_f32_16x16x32_bf16 v[52:55], v[148:151], v[156:159], v[52:55]\n\t" \
  "v_mfma_f32_16x16x32_bf16 v[72:75], v[148:151], v[160:163], v[72:75]\n\t" \
  "v_mfma_f32_16x16x32_bf16 v[92:95], v[148:151], v[164:167], v[92:95]\n\t" \
  "global_load_dwordx4 v[132:135], v240, s[60:61] sc1\n\t" \
  "global_load_dwordx4 v[136:139], v240, s[60:61] offset:1024 sc1\n\t" \
  "global_load_dwordx4 v[140:143], v240, s[60:61] offset:2048 sc1\n\t" \
  "global_load_dwordx4 v[144:147], v240, s[60:61] offset:3072 sc1\n\t" \
  "global_load_dwordx4 v[148:151], v245, s[60:61] sc1\n\t" \
  "global_load_dwordx4 v[152:155], v241, s[62:63] sc1\n\t" \
  "global_load_dwordx4 v[156:159], v242, s[62:63] sc1\n\t" \
  "global_load_dwordx4 v[160:163], v243, s[62:63] sc1\n\t" \
  "global_load_dwordx4 v[164:167], v244, s[62:63] sc1\n\t" \
  "s_add_u32 s60, s60, 5120\n\t" \
  "s_addc_u32 s61, s61, 0\n\t" \
  "s_add_u32 s62, s62, 1024\n\t" \
  "s_addc_u32 s63, s63, 0\n\t" \
  "s_waitcnt vmcnt(27)\n\t" \
  "v_mfma_f32_16x16x32_bf16 v[16:19], v[168:171], v[188:191], v[16:19]\n\t" \
  "v_mfma_f32_16x16x32_bf16 v[36:39], v[168:171], v[192:195], v[36:39]\n\t" \
  "v_mfma_f32_16x16x32_bf16 v[56:59], v[168:171], v[196:199], v[56:59]\n\t" \
  "v_mfma_f32_16x16x32_bf16 v[76:79], v[168:171], v[200:203], v[76:79]\n\t" \
  "v_mfma_f32_16x16x32_bf16 v[20:23], v[172:175], v[188:191], v[20:23]\n\t" \
  "v_mfma_f32_16x16x32_bf16 v[40:43], v[172:175], v[192:195], v[40:43]\n\t" \
  "v_mfma_f32_16x16x32_bf16 v[60:63], v[172:175], v[196:199], v[60:63]\n\t" \
  "v_mfma_f32_16x16x32_bf16 v[80:83], v[172:175], v[200:203], v[80:83]\n\t" \
  "v_mfma_f32_16x16x32_bf16 v[24:27], v[176:179], v[188:191], v[24:27]\n\t" \
  "v_mfma_f32_16x16x32_bf16 v[44:47], v[176:179], v[192:195], v[44:47]\n\t" \
  "v_mfma_f32_16x16x32_bf16 v[64:67], v[176:179], v[196:199], v[64:67]\n\t" \
  "v_mfma_f32_16x16x32_bf16 v[84:87], v[176:179], v[200:203], v[84:87]\n\t" \
  "v_mfma_f32_16x16x32_bf16 v[28:31], v[180:183], v[188:191], v[28:31]\n\t" \
  "v_mfma_f32_16x16x32_bf16 v[48:51], v[180:183], v[192:195], v[48:51]\n\t" \
  "v_mfma_f32_16x16x32_bf16 v[68:71], v[180:183], v[196:199], v[68:71]\n\t" \
  "v_mfma_f32_16x16x32_bf16 v[88:91], v[180:183], v[200:203], v[88:91]\n\t" \
  "v_mfma_f32_16x16x32_bf16 v[32:35], v[184:187], v[188:191], v[32:35]\n\t" \
  "v_mfma_f32_16x16x32_bf16 v[52:55], v[184:187], v[192:195], v[52:55]\n\t" \
  "v_mfma_f32_16x16x32_bf16 v[72:75], v[184:187], v[196:199], v[72:75]\n\t" \
  "v_mfma_f32_16x16x32_bf16 v[92:95], v[184:187], v[200:203], v[92:95]\n\t" \
  "global_load_dwordx4 v[168:171], v240, s[60:61] sc1\n\t" \
  "global_load_dwordx4 v[172:175], v240, s[60:61] offset:1024 sc1\n\t" \
  "global_load_dwordx4 v[176:179], v240, s[60:61] offset:2048 sc1\n\t" \
  "global_load_dwordx4 v[180:183], v240, s[60:61] offset:3072 sc1\n\t" \
  "global_load_dwordx4 v[184:187], v245, s[60:61] sc1\n\t" \
  "global_load_dwordx4 v[188:191], v241, s[62:63] sc1\n\t" \
  "global_load_dwordx4 v[192:195], v242, s[62:63] sc1\n\t" \
  "global_load_dwordx4 v[196:199], v243, s[62:63] sc1\n\t" \
  "global_load_dwordx4 v[200:203], v244, s[62:63] sc1\n\t" \
  "s_add_u32 s60, s60, 5120\n\t" \
  "s_addc_u32 s61, s61, 0\n\t" \
  "s_add_u32 s62, s62, 1024\n\t" \
  "s_addc_u32 s63, s63, 0\n\t" \
  "s_waitcnt vmcnt(27)\n\t" \
  "v_mfma_f32_16x16x32_bf16 v[16:19], v[204:207], v[224:227], v[16:19]\n\t" \
  "v_mfma_f32_16x16x32_bf16 v[36:39], v[204:207], v[228:231], v[36:39]\n\t" \
  "v_mfma_f32_16x16x32_bf16 v[56:59], v[204:207], v[232:235], v[56:59]\n\t" \
  "v_mfma_f32_16x16x32_bf16 v[76:79], v[204:207], v[236:239], v[76:79]\n\t" \
  "v_mfma_f32_16x16x32_bf16 v[20:23], v[208:211], v[224:227], v[20:23]\n\t" \
  "v_mfma_f32_16x16x32_bf16 v[40:43], v[208:211], v[228:231], v[40:43]\n\t" \
  "v_mfma_f32_16x16x32_bf16 v[60:63], v[208:211], v[232:235], v[60:63]\n\t" \
  "v_mfma_f32_16x16x32_bf16 v[80:83], v[208:211], v[236:239], v[80:83]\n\t" \
  "v_mfma_f32_16x16x32_bf16 v[24:27], v[212:215], v[224:227], v[24:27]\n\t" \
  "v_mfma_f32_16x16x32_bf16 v[44:47], v[212:215], v[228:231], v[44:47]\n\t" \
  "v_mfma_f32_16x16x32_bf16 v[64:67], v[212:215], v[232:235], v[64:67]\n\t" \
  "v_mfma_f32_16x16x32_bf16 v[84:87], v[212:215], v[236:239], v[84:87]\n\t" \
  "v_mfma_f32_16x16x32_bf16 v[28:31], v[216:219], v[224:227], v[28:31]\n\t" \
  "v_mfma_f32_16x16x32_bf16 v[48:51], v[216:219], v[228:231], v[48:51]\n\t" \
  "v_mfma_f32_16x16x32_bf16 v[68:71], v[216:219], v[232:235], v[68:71]\n\t" \
  "v_mfma_f32_16x16x32_bf16 v[88:91], v[216:219], v[236:239], v[88:91]\n\t" \
  "v_mfma_f32_16x16x32_bf16 v[32:35], v[220:223], v[224:227], v[32:35]\n\t" \
  "v_mfma_f32_16x16x32_bf16 v[52:55], v[220:223], v[228:231], v[52:55]\n\t" \
  "v_mfma_f32_16x16x32_bf16 v[72:75], v[220:223], v[232:235], v[72:75]\n\t" \
  "v_mfma_f32_16x16x32_bf16 v[92:95], v[220:223], v[236:239], v[92:95]\n\t" \
  "global_load_dwordx4 v[204:207], v240, s[60:61] sc1\n\t" \
  "global_load_dwordx4 v[208:211], v240, s[60:61] offset:1024 sc1\n\t" \
  "global_load_dwordx4 v[212:215], v240, s[60:61] offset:2048 sc1\n\t" \
  "global_load_dwordx4 v[216:219], v240, s[60:61] offset:3072 sc1\n\t" \
  "global_load_dwordx4 v[220:223], v245, s[60:61] sc1\n\t" \
  "global_load_dwordx4 v[224:227], v241, s[62:63] sc1\n\t" \
  "global_load_dwordx4 v[228:231], v242, s[62:63] sc1\n\t" \
  "global_load_dwordx4 v[232:235], v243, s[62:63] sc1\n\t" \
  "global_load_dwordx4 v[236:239], v244, s[62:63] sc1\n\t" \
  "s_add_u32 s60, s60, 5120\n\t" \
  "s_addc_u32 s61, s61, 0\n\t" \
  "s_add_u32 s62, s62, 1024\n\t" \
  "s_addc_u32 s63, s63, 0\n\t" \
  "s_waitcnt vmcnt(27)\n\t" \
  "v_mfma_f32_16x16x32_bf16 v[16:19], v[96:99], v[116:119], v[16:19]\n\t" \
  "v_mfma_f32_16x16x32_bf16 v[36:39], v[96:99], v[120:123], v[36:39]\n\t" \
  "v_mfma_f32_16x16x32_bf16 v[56:59], v[96:99], v[124:127], v[56:59]\n\t" \
  "v_mfma_f32_16x16x32_bf16 v[76:79], v[96:99], v[128:131], v[76:79]\n\t" \
  "v_mfma_f32_16x16x32_bf16 v[20:23], v[100:103], v[116:119], v[20:23]\n\t" \
  "v_mfma_f32_16x16x32_bf16 v[40:43], v[100:103], v[120:123], v[40:43]\n\t" \
  "v_mfma_f32_16x16x32_bf16 v[60:63], v[100:103], v[124:127], v[60:63]\n\t" \
  "v_mfma_f32_16x16x32_bf16 v[80:83], v[100:103], v[128:131], v[80:83]\n\t" \
  "v_mfma_f32_16x16x32_bf16 v[24:27], v[104:107], v[116:119], v[24:27]\n\t" \
  "v_mfma_f32_16x16x32_bf16 v[44:47], v[104:107], v[120:123], v[44:47]\n\t" \
  "v_mfma_f32_16x16x32_bf16 v[64:67], v[104:107], v[124:127], v[64:67]\n\t" \
  "v_mfma_f32_16x16x32_bf16 v[84:87], v[104:107], v[128:131], v[84:87]\n\t" \
  "v_mfma_f32_16x16x32_bf16 v[28:31], v[108:111], v[116:119], v[28:31]\n\t" \
  "v_mfma_f32_16x16x32_bf16 v[48:51], v[108:111], v[120:123], v[48:51]\n\t" \
  "v_mfma_f32_16x16x32_bf16 v[68:71], v[108:111], v[124:127], v[68:71]\n\t" \
  "v_mfma_f32_16x16x32_bf16 v[88:91], v[108:111], v[128:131], v[88:91]\n\t" \
  "v_mfma_f32_16x16x32_bf16 v[32:35], v[112:115], v[116:119], v[32:35]\n\t" \
  "v_mfma_f32_16x16x32_bf16 v[52:55], v[112:115], v[120:123], v[52:55]\n\t" \
  "v_mfma_f32_16x16x32_bf16 v[72:75], v[112:115], v[124:127], v[72:75]\n\t" \
  "v_mfma_f32_16x16x32_bf16 v[92:95], v[112:115], v[128:131], v[92:95]\n\t" \
  "global_load_dwordx4 v[96:99], v240, s[60:61] sc1\n\t" \
  "global_load_dwordx4 v[100:103], v240, s[60:61] offset:1024 sc1\n\t" \
  "global_load_dwordx4 v[104:107], v240, s[60:61] offset:2048 sc1\n\t" \
  "global_load_dwordx4 v[108:111], v240, s[60:61] offset:3072 sc1\n\t" \
  "global_load_dwordx4 v[112:115], v245, s[60:61] sc1\n\t" \
  "global_load_dwordx4 v[116:119], v241, s[62:63] sc1\n\t" \
  "global_load_dwordx4 v[120:123], v242, s[62:63] sc1\n\t" \
  "global_load_dwordx4 v[124:127], v243, s[62:63] sc1\n\t" \
  "global_load_dwordx4 v[128:131], v244, s[62:63] sc1\n\t" \
  "s_add_u32 s60, s60, 5120\n\t" \
  "s_addc_u32 s61, s61, 0\n\t" \
  "s_add_u32 s62, s62, 1024\n\t" \
  "s_addc_u32 s63, s63, 0\n\t" \
  "s_waitcnt vmcnt(27)\n\t" \
  "v_mfma_f32_16x16x32_bf16 v[16:19], v[132:135], v[152:155], v[16:19]\n\t" \
  "v_mfma_f32_16x16x32_bf16 v[36:39], v[132:135], v[156:159], v[36:39]\n\t" \
  "v_mfma_f32_16x16x32_bf16 v[56:59], v[132:135], v[160:163], v[56:59]\n\t" \
  "v_mfma_f32_16x16x32_bf16 v[76:79], v[132:135], v[164:167], v[76:79]\n\t" \
  "v_mfma_f32_16x16x32_bf16 v[20:23], v[136:139], v[152:155], v[20:23]\n\t" \
  "v_mfma_f32_16x16x32_bf16 v[40:43], v[136:139], v[156:159], v[40:43]\n\t" \
  "v_mfma_f32_16x16x32_bf16 v[60:63], v[136:139], v[160:163], v[60:63]\n\t" \
  "v_mfma_f32_16x16x32_bf16 v[80:83], v[136:139], v[164:167], v[80:83]\n\t" \
  "v_mfma_f32_16x16x32_bf16 v[24:27], v[140:143], v[152:155], v[24:27]\n\t" \
  "v_mfma_f32_16x16x32_bf16 v[44:47], v[140:143], v[156:159], v[44:47]\n\t" \
  "v_mfma_f32_16x16x32_bf16 v[64:67], v[140:143], v[160:163], v[64:67]\n\t" \
  "v_mfma_f32_16x16x32_bf16 v[84:87], v[140:143], v[164:167], v[84:87]\n\t" \
  "v_mfma_f32_16x16x32_bf16 v[28:31], v[144:147], v[152:155], v[28:31]\n\t" \
  "v_mfma_f32_16x16x32_bf16 v[48:51], v[144:147], v[156:159], v[48:51]\n\t" \
  "v_mfma_f32_16x16x32_bf16 v[68:71], v[144:147], v[160:163], v[68:71]\n\t" \
  "v_mfma_f32_16x16x32_bf16 v[88:91], v[144:147], v[164:167], v[88:91]\n\t" \
  "v_mfma_f32_16x16x32_bf16 v[32:35], v[148:151], v[152:155], v[32:35]\n\t" \
  "v_mfma_f32_16x16x32_bf16 v[52:55], v[148:151], v[156:159], v[52:55]\n\t" \
  "v_mfma_f32_16x16x32_bf16 v[72:75], v[148:151], v[160:163], v[72:75]\n\t" \
  "v_mfma_f32_16x16x32_bf16 v[92:95], v[148:151], v[164:167], v[92:95]\n\t" \
  "global_load_dwordx4 v[132:135], v240, s[60:61] sc1\n\t" \
  "global_load_dwordx4 v[136:139], v240, s[60:61] offset:1024 sc1\n\t" \
  "global_load_dwordx4 v[140:143], v240, s[60:61] offset:2048 sc1\n\t" \
  "global_load_dwordx4 v[144:147], v240, s[60:61] offset:3072 sc1\n\t" \
  "global_load_dwordx4 v[148:151], v245, s[60:61] sc1\n\t" \
  "global_load_dwordx4 v[152:155], v241, s[62:63] sc1\n\t" \
  "global_load_dwordx4 v[156:159], v242, s[62:63] sc1\n\t" \
  "global_load_dwordx4 v[160:163], v243, s[62:63] sc1\n\t" \
  "global_load_dwordx4 v[164:167], v244, s[62:63] sc1\n\t" \
  "s_add_u32 s60, s60, 5120\n\t" \
  "s_addc_u32 s61, s61, 0\n\t" \
  "s_add_u32 s62, s62, 1024\n\t" \
  "s_addc_u32 s63, s63, 0\n\t" \
  "s_waitcnt vmcnt(27)\n\t" \
  "v_mfma_f32_16x16x32_bf16 v[16:19], v[168:171], v[188:191], v[16:19]\n\t" \
  "v_mfma_f32_16x16x32_bf16 v[36:39], v[168:171], v[192:195], v[36:39]\n\t" \
  "v_mfma_f32_16x16x32_bf16 v[56:59], v[168:171], v[196:199], v[56:59]\n\t" \
  "v_mfma_f32_16x16x32_bf16 v[76:79], v[168:171], v[200:203], v[76:79]\n\t" \
  "v_mfma_f32_16x16x32_bf16 v[20:23], v[172:175], v[188:191], v[20:23]\n\t" \
  "v_mfma_f32_16x16x32_bf16 v[40:43], v[172:175], v[192:195], v[40:43]\n\t" \
  "v_mfma_f32_16x16x32_bf16 v[60:63], v[172:175], v[196:199], v[60:63]\n\t" \
  "v_mfma_f32_16x16x32_bf16 v[80:83], v[172:175], v[200:203], v[80:83]\n\t" \
  "v_mfma_f32_16x16x32_bf16 v[24:27], v[176:179], v[188:191], v[24:27]\n\t" \
  "v_mfma_f32_16x16x32_bf16 v[44:47], v[176:179], v[192:195], v[44:47]\n\t" \
  "v_mfma_f32_16x16x32_bf16 v[64:67], v[176:179], v[196:199], v[64:67]\n\t" \
  "v_mfma_f32_16x16x32_bf16 v[84:87], v[176:179], v[200:203], v[84:87]\n\t" \
  "v_mfma_f32_16x16x32_bf16 v[28:31], v[180:183], v[188:191], v[28:31]\n\t" \
  "v_mfma_f32_16x16x32_bf16 v[48:51], v[180:183], v[192:195], v[48:51]\n\t" \
  "v_mfma_f32_16x16x32_bf16 v[68:71], v[180:183], v[196:199], v[68:71]\n\t" \
  "v_mfma_f32_16x16x32_bf16 v[88:91], v[180:183], v[200:203], v[88:91]\n\t" \
  "v_mfma_f32_16x16x32_bf16 v[32:35], v[184:187], v[188:191], v[32:35]\n\t" \
  "v_mfma_f32_16x16x32_bf16 v[52:55], v[184:187], v[192:195], v[52:55]\n\t" \
  "v_mfma_f32_16x16x32_bf16 v[72:75], v[184:187], v[196:199], v[72:75]\n\t" \
  "v_mfma_f32_16x16x32_bf16 v[92:95], v[184:187], v[200:203], v[92:95]\n\t" \
  "s_waitcnt vmcnt(18)\n\t" \
  "v_mfma_f32_16x16x32_bf16 v[16:19], v[204:207], v[224:227], v[16:19]\n\t" \
  "v_mfma_f32_16x16x32_bf16 v[36:39], v[204:207], v[228:231], v[36:39]\n\t" \
  "v_mfma_f32_16x16x32_bf16 v[56:59], v[204:207], v[232:235], v[56:59]\n\t" \
  "v_mfma_f32_16x16x32_bf16 v[76:79], v[204:207], v[236:239], v[76:79]\n\t" \
  "v_mfma_f32_16x16x32_bf16 v[20:23], v[208:211], v[224:227], v[20:23]\n\t" \
  "v_mfma_f32_16x16x32_bf16 v[40:43], v[208:211], v[228:231], v[40:43]\n\t" \
  "v_mfma_f32_16x16x32_bf16 v[60:63], v[208:211], v[232:235], v[60:63]\n\t" \
  "v_mfma_f32_16x16x32_bf16 v[80:83], v[208:211], v[236:239], v[80:83]\n\t" \
  "v_mfma_f32_16x16x32_bf16 v[24:27], v[212:215], v[224:227], v[24:27]\n\t" \
  "v_mfma_f32_16x16x32_bf16 v[44:47], v[212:215], v[228:231], v[44:47]\n\t" \
  "v_mfma_f32_16x16x32_bf16 v[64:67], v[212:215], v[232:235], v[64:67]\n\t" \
  "v_mfma_f32_16x16x32_bf16 v[84:87], v[212:215], v[236:239], v[84:87]\n\t" \
  "v_mfma_f32_16x16x32_bf16 v[28:31], v[216:219], v[224:227], v[28:31]\n\t" \
  "v_mfma_f32_16x16x32_bf16 v[48:51], v[216:219], v[228:231], v[48:51]\n\t" \
  "v_mfma_f32_16x16x32_bf16 v[68:71], v[216:219], v[232:235], v[68:71]\n\t" \
  "v_mfma_f32_16x16x32_bf16 v[88:91], v[216:219], v[236:239], v[88:91]\n\t" \
  "v_mfma_f32_16x16x32_bf16 v[32:35], v[220:223], v[224:227], v[32:35]\n\t" \
  "v_mfma_f32_16x16x32_bf16 v[52:55], v[220:223], v[228:231], v[52:55]\n\t" \
  "v_mfma_f32_16x16x32_bf16 v[72:75], v[220:223], v[232:235], v[72:75]\n\t" \
  "v_mfma_f32_16x16x32_bf16 v[92:95], v[220:223], v[236:239], v[92:95]\n\t" \
  "s_waitcnt vmcnt(9)\n\t" \
  "v_mfma_f32_16x16x32_bf16 v[16:19], v[96:99], v[116:119], v[16:19]\n\t" \
  "v_mfma_f32_16x16x32_bf16 v[36:39], v[96:99], v[120:123], v[36:39]\n\t" \
  "v_mfma_f32_16x16x32_bf16 v[56:59], v[96:99], v[124:127], v[56:59]\n\t" \
  "v_mfma_f32_16x16x32_bf16 v[76:79], v[96:99], v[128:131], v[76:79]\n\t" \
  "v_mfma_f32_16x16x32_bf16 v[20:23], v[100:103], v[116:119], v[20:23]\n\t" \
  "v_mfma_f32_16x16x32_bf16 v[40:43], v[100:103], v[120:123], v[40:43]\n\t" \
  "v_mfma_f32_16x16x32_bf16 v[60:63], v[100:103], v[124:127], v[60:63]\n\t" \
  "v_mfma_f32_16x16x32_bf16 v[80:83], v[100:103], v[128:131], v[80:83]\n\t" \
  "v_mfma_f32_16x16x32_bf16 v[24:27], v[104:107], v[116:119], v[24:27]\n\t" \
  "v_mfma_f32_16x16x32_bf16 v[44:47], v[104:107], v[120:123], v[44:47]\n\t" \
  "v_mfma_f32_16x16x32_bf16 v[64:67], v[104:107], v[124:127], v[64:67]\n\t" \
  "v_mfma_f32_16x16x32_bf16 v[84:87], v[104:107], v[128:131], v[84:87]\n\t" \
  "v_mfma_f32_16x16x32_bf16 v[28:31], v[108:111], v[116:119], v[28:31]\n\t" \
  "v_mfma_f32_16x16x32_bf16 v[48:51], v[108:111], v[120:123], v[48:51]\n\t" \
  "v_mfma_f32_16x16x32_bf16 v[68:71], v[108:111], v[124:127], v[68:71]\n\t" \
  "v_mfma_f32_16x16x32_bf16 v[88:91], v[108:111], v[128:131], v[88:91]\n\t" \
  "v_mfma_f32_16x16x32_bf16 v[32:35], v[112:115], v[116:119], v[32:35]\n\t" \
  "v_mfma_f32_16x16x32_bf16 v[52:55], v[112:115], v[120:123], v[52:55]\n\t" \
  "v_mfma_f32_16x16x32_bf16 v[72:75], v[112:115], v[124:127], v[72:75]\n\t" \
  "v_mfma_f32_16x16x32_bf16 v[92:95], v[112:115], v[128:131], v[92:95]\n\t" \
  "s_waitcnt vmcnt(0)\n\t" \
  "s_cmp_eq_u32 s69, 0\n\t" \
  "s_cbranch_scc1 DOWN_END%=\n\t" \
  "v_mfma_f32_16x16x32_bf16 v[16:19], v[132:135], v[152:155], v[16:19]\n\t" \
  "v_mfma_f32_16x16x32_bf16 v[36:39], v[132:135], v[156:159], v[36:39]\n\t" \
  "v_mfma_f32_16x16x32_bf16 v[56:59], v[132:135], v[160:163], v[56:59]\n\t" \
  "v_mfma_f32_16x16x32_bf16 v[76:79], v[132:135], v[164:167], v[76:79]\n\t" \
  "v_mfma_f32_16x16x32_bf16 v[20:23], v[136:139], v[152:155], v[20:23]\n\t" \
  "v_mfma_f32_16x16x32_bf16 v[40:43], v[136:139], v[156:159], v[40:43]\n\t" \
  "v_mfma_f32_16x16x32_bf16 v[60:63], v[136:139], v[160:163], v[60:63]\n\t" \
  "v_mfma_f32_16x16x32_bf16 v[80:83], v[136:139], v[164:167], v[80:83]\n\t" \
  "v_mfma_f32_16x16x32_bf16 v[24:27], v[140:143], v[152:155], v[24:27]\n\t" \
  "v_mfma_f32_16x16x32_bf16 v[44:47], v[140:143], v[156:159], v[44:47]\n\t" \
  "v_mfma_f32_16x16x32_bf16 v[64:67], v[140:143], v[160:163], v[64:67]\n\t" \
  "v_mfma_f32_16x16x32_bf16 v[84:87], v[140:143], v[164:167], v[84:87]\n\t" \
  "v_mfma_f32_16x16x32_bf16 v[28:31], v[144:147], v[152:155], v[28:31]\n\t" \
  "v_mfma_f32_16x16x32_bf16 v[48:51], v[144:147], v[156:159], v[48:51]\n\t" \
  "v_mfma_f32_16x16x32_bf16 v[68:71], v[144:147], v[160:163], v[68:71]\n\t" \
  "v_mfma_f32_16x16x32_bf16 v[88:91], v[144:147], v[164:167], v[88:91]\n\t" \
  "v_mfma_f32_16x16x32_bf16 v[32:35], v[148:151], v[152:155], v[32:35]\n\t" \
  "v_mfma_f32_16x16x32_bf16 v[52:55], v[148:151], v[156:159], v[52:55]\n\t" \
  "v_mfma_f32_16x16x32_bf16 v[72:75], v[148:151], v[160:163], v[72:75]\n\t" \
  "v_mfma_f32_16x16x32_bf16 v[92:95], v[148:151], v[164:167], v[92:95]\n\t" \
  "DOWN_END%=:\n\t" \
  "s_nop 15\n\t" \
  ""
#define WO3_ASM \
  "global_load_dwordx4 v[96:99], v240, s[60:61] sc1\n\t" \
  "global_load_dwordx4 v[100:103], v240, s[60:61] offset:1024 sc1\n\t" \
  "global_load_dwordx4 v[104:107], v240, s[60:61] offset:2048 sc1\n\t" \
  "global_load_dwordx4 v[108:111], v240, s[60:61] offset:3072 sc1\n\t" \
  "global_load_dwordx4 v[112:115], v245, s[60:61] sc1\n\t" \
  "s_add_u32 s60, s60, 5120\n\t" \
  "s_addc_u32 s61, s61, 0\n\t" \
  "global_load_dwordx4 v[116:119], v240, s[60:61] sc1\n\t" \
  "global_load_dwordx4 v[120:123], v240, s[60:61] offset:1024 sc1\n\t" \
  "global_load_dwordx4 v[124:127], v240, s[60:61] offset:2048 sc1\n\t" \
  "global_load_dwordx4 v[128:131], v240, s[60:61] offset:3072 sc1\n\t" \
  "global_load_dwordx4 v[132:135], v245, s[60:61] sc1\n\t" \
  "s_add_u32 s60, s60, 5120\n\t" \
  "s_addc_u32 s61, s61, 0\n\t" \
  "global_load_dwordx4 v[136:139], v240, s[60:61] sc1\n\t" \
  "global_load_dwordx4 v[140:143], v240, s[60:61] offset:1024 sc1\n\t" \
  "global_load_dwordx4 v[144:147], v240, s[60:61] offset:2048 sc1\n\t" \
  "global_load_dwordx4 v[148:151], v240, s[60:61] offset:3072 sc1\n\t" \
  "global_load_dwordx4 v[152:155], v245, s[60:61] sc1\n\t" \
  "s_add_u32 s60, s60, 5120\n\t" \
  "s_addc_u32 s61, s61, 0\n\t" \
  "s_mov_b32 s64, 0\n\t" \
  "WO_POLL%=:\n\t" \
  "global_load_dword v12, v241, s[62:63] sc1\n\t" \
  "global_load_dword v13, v241, s[66:67] sc1\n\t" \
  "s_waitcnt vmcnt(0)\n\t" \
  "v_cmp_eq_u32 vcc, -1, v12\n\t" \
  "v_cmp_eq_u32 s[70:71], -1, v13\n\t" \
  "s_nop 3\n\t" \
  "s_or_b64 s[70:71], s[70:71], vcc\n\t" \
  "s_cbranch_scc0 WO_GO%=\n\t" \
  "s_sleep 2\n\t" \
  "s_add_u32 s64, s64, 1\n\t" \
  "s_cmp_lt_u32 s64, 0x40000\n\t" \
  "s_cbranch_scc1 WO_POLL%=\n\t" \
  "WO_GO%=:\n\t" \
  "s_mov_b64 s[72:73], s[62:63]\n\t" \
  "WO_RETRY%=:\n\t" \
  "s_mov_b64 s[62:63], s[72:73]\n\t" \
  "global_load_dwordx4 v[156:159], v241, s[62:63] sc1\n\t" \
  "global_load_dwordx4 v[160:163], v242, s[62:63] sc1\n\t" \
  "global_load_dwordx4 v[164:167], v243, s[62:63] sc1\n\t" \
  "global_load_dwordx4 v[168:171], v244, s[62:63] sc1\n\t" \
  "s_add_u32 s62, s62, 1024\n\t" \
  "s_addc_u32 s63, s63, 0\n\t" \
  "global_load_dwordx4 v[172:175], v241, s[62:63] sc1\n\t" \
  "global_load_dwordx4 v[176:179], v242, s[62:63] sc1\n\t" \
  "global_load_dwordx4 v[180:183], v243, s[62:63] sc1\n\t" \
  "global_load_dwordx4 v[184:187], v244, s[62:63] sc1\n\t" \
  "s_add_u32 s62, s62, 1024\n\t" \
  "s_addc_u32 s63, s63, 0\n\t" \
  "global_load_dwordx4 v[188:191], v241, s[62:63] sc1\n\t" \
  "global_load_dwordx4 v[192:195], v242, s[62:63] sc1\n\t" \
  "global_load_dwordx4 v[196:199], v243, s[62:63] sc1\n\t" \
  "global_load_dwordx4 v[200:203], v244, s[62:63] sc1\n\t" \
  "s_add_u32 s62, s62, 1024\n\t" \
  "s_addc_u32 s63, s63, 0\n\t" \
  "global_load_dwordx4 v[204:207], v241, s[62:63] sc1\n\t" \
  "global_load_dwordx4 v[208:211], v242, s[62:63] sc1\n\t" \
  "global_load_dwordx4 v[212:215], v243, s[62:63] sc1\n\t" \
  "global_load_dwordx4 v[216:219], v244, s[62:63] sc1\n\t" \
  "s_add_u32 s62, s62, 1024\n\t" \
  "s_addc_u32 s63, s63, 0\n\t" \
  "global_load_dwordx4 v[220:223], v241, s[62:63] sc1\n\t" \
  "global_load_dwordx4 v[224:227], v242, s[62:63] sc1\n\t" \
  "global_load_dwordx4 v[228:231], v243, s[62:63] sc1\n\t" \
  "global_load_dwordx4 v[232:235], v244, s[62:63] sc1\n\t" \
  "s_add_u32 s62, s62, 1024\n\t" \
  "s_addc_u32 s63, s63, 0\n\t" \
  "s_waitcnt vmcnt(0)\n\t" \
  "s_mov_b64 s[70:71], 0\n\t" \
  "v_cmp_eq_u32 vcc, -1, v156\n\t" \
  "s_or_b64 s[70:71], s[70:71], vcc\n\t" \
  "v_cmp_eq_u32 vcc, -1, v159\n\t" \
  "s_or_b64 s[70:71], s[70:71], vcc\n\t" \
  "v_cmp_eq_u32 vcc, -1, v160\n\t" \
  "s_or_b64 s[70:71], s[70:71], vcc\n\t" \
  "v_cmp_eq_u32 vcc, -1, v163\n\t" \
  "s_or_b64 s[70:71], s[70:71], vcc\n\t" \
  "v_cmp_eq_u32 vcc, -1, v164\n\t" \
  "s_or_b64 s[70:71], s[70:71], vcc\n\t" \
  "v_cmp_eq_u32 vcc, -1, v167\n\t" \
  "s_or_b64 s[70:71], s[70:71], vcc\n\t" \
  "v_cmp_eq_u32 vcc, -1, v168\n\t" \
  "s_or_b64 s[70:71], s[70:71], vcc\n\t" \
  "v_cmp_eq_u32 vcc, -1, v171\n\t" \
  "s_or_b64 s[70:71], s[70:71], vcc\n\t" \
  "v_cmp_eq_u32 vcc, -1, v172\n\t" \
  "s_or_b64 s[70:71], s[70:71], vcc\n\t" \
  "v_cmp_eq_u32 vcc, -1, v175\n\t" \
  "s_or_b64 s[70:71], s[70:71], vcc\n\t" \
  "v_cmp_eq_u32 vcc, -1, v176\n\t" \
  "s_or_b64 s[70:71], s[70:71], vcc\n\t" \
  "v_cmp_eq_u32 vcc, -1, v179\n\t" \
  "s_or_b64 s[70:71], s[70:71], vcc\n\t" \
  "v_cmp_eq_u32 vcc, -1, v180\n\t" \
  "s_or_b64 s[70:71], s[70:71], vcc\n\t" \
  "v_cmp_eq_u32 vcc, -1, v183\n\t" \
  "s_or_b64 s[70:71], s[70:71], vcc\n\t" \
  "v_cmp_eq_u32 vcc, -1, v184\n\t" \
  "s_or_b64 s[70:71], s[70:71], vcc\n\t" \
  "v_cmp_eq_u32 vcc, -1, v187\n\t" \
  "s_or_b64 s[70:71], s[70:71], vcc\n\t" \
  "v_cmp_eq_u32 vcc, -1, v188\n\t" \
  "s_or_b64 s[70:71], s[70:71], vcc\n\t" \
  "v_cmp_eq_u32 vcc, -1, v191\n\t" \
  "s_or_b64 s[70:71], s[70:71], vcc\n\t" \
  "v_cmp_eq_u32 vcc, -1, v192\n\t" \
  "s_or_b64 s[70:71], s[70:71], vcc\n\t" \
  "v_cmp_eq_u32 vcc, -1, v195\n\t" \
  "s_or_b64 s[70:71], s[70:71], vcc\n\t" \
  "v_cmp_eq_u32 vcc, -1, v196\n\t" \
  "s_or_b64 s[70:71], s[70:71], vcc\n\t" \
  "v_cmp_eq_u32 vcc, -1, v199\n\t" \
  "s_or_b64 s[70:71], s[70:71], vcc\n\t" \
  "v_cmp_eq_u32 vcc, -1, v200\n\t" \
  "s_or_b64 s[70:71], s[70:71], vcc\n\t" \
  "v_cmp_eq_u32 vcc, -1, v203\n\t" \
  "s_or_b64 s[70:71], s[70:71], vcc\n\t" \
  "v_cmp_eq_u32 vcc, -1, v204\n\t" \
  "s_or_b64 s[70:71], s[70:71], vcc\n\t" \
  "v_cmp_eq_u32 vcc, -1, v207\n\t" \
  "s_or_b64 s[70:71], s[70:71], vcc\n\t" \
  "v_cmp_eq_u32 vcc, -1, v208\n\t" \
  "s_or_b64 s[70:71], s[70:71], vcc\n\t" \
  "v_cmp_eq_u32 vcc, -1, v211\n\t" \
  "s_or_b64 s[70:71], s[70:71], vcc\n\t" \
  "v_cmp_eq_u32 vcc, -1, v212\n\t" \
  "s_or_b64 s[70:71], s[70:71], vcc\n\t" \
  "v_cmp_eq_u32 vcc, -1, v215\n\t" \
  "s_or_b64 s[70:71], s[70:71], vcc\n\t" \
  "v_cmp_eq_u32 vcc, -1, v216\n\t" \
  "s_or_b64 s[70:71], s[70:71], vcc\n\t" \
  "v_cmp_eq_u32 vcc, -1, v219\n\t" \
  "s_or_b64 s[70:71], s[70:71], vcc\n\t" \
  "v_cmp_eq_u32 vcc, -1, v220\n\t" \
  "s_or_b64 s[70:71], s[70:71], vcc\n\t" \
  "v_cmp_eq_u32 vcc, -1, v223\n\t" \
  "s_or_b64 s[70:71], s[70:71], vcc\n\t" \
  "v_cmp_eq_u32 vcc, -1, v224\n\t" \
  "s_or_b64 s[70:71], s[70:71], vcc\n\t" \
  "v_cmp_eq_u32 vcc, -1, v227\n\t" \
  "s_or_b64 s[70:71], s[70:71], vcc\n\t" \
  "v_cmp_eq_u32 vcc, -1, v228\n\t" \
  "s_or_b64 s[70:71], s[70:71], vcc\n\t" \
  "v_cmp_eq_u32 vcc, -1, v231\n\t" \
  "s_or_b64 s[70:71], s[70:71], vcc\n\t" \
  "v_cmp_eq_u32 vcc, -1, v232\n\t" \
  "s_or_b64 s[70:71], s[70:71], vcc\n\t" \
  "v_cmp_eq_u32 vcc, -1, v235\n\t" \
  "s_or_b64 s[70:71], s[70:71], vcc\n\t" \
  "s_cmp_eq_u64 s[70:71], 0\n\t" \
  "s_cbranch_scc1 WO_RUN%=\n\t" \
  "s_sleep 1\n\t" \
  "s_add_u32 s64, s64, 1\n\t" \
  "s_cmp_lt_u32 s64, 0x40000\n\t" \
  "s_cbranch_scc1 WO_RETRY%=\n\t" \
  "WO_RUN%=:\n\t" \
  "v_mfma_f32_16x16x32_bf16 v[16:19], v[96:99], v[156:159], v[16:19]\n\t" \
  "v_mfma_f32_16x16x32_bf16 v[36:39], v[96:99], v[160:163], v[36:39]\n\t" \
  "v_mfma_f32_16x16x32_bf16 v[56:59], v[96:99], v[164:167], v[56:59]\n\t" \
  "v_mfma_f32_16x16x32_bf16 v[76:79], v[96:99], v[168:171], v[76:79]\n\t" \
  "v_mfma_f32_16x16x32_bf16 v[20:23], v[100:103], v[156:159], v[20:23]\n\t" \
  "v_mfma_f32_16x16x32_bf16 v[40:43], v[100:103], v[160:163], v[40:43]\n\t" \
  "v_mfma_f32_16x16x32_bf16 v[60:63], v[100:103], v[164:167], v[60:63]\n\t" \
  "v_mfma_f32_16x16x32_bf16 v[80:83], v[100:103], v[168:171], v[80:83]\n\t" \
  "v_mfma_f32_16x16x32_bf16 v[24:27], v[104:107], v[156:159], v[24:27]\n\t" \
  "v_mfma_f32_16x16x32_bf16 v[44:47], v[104:107], v[160:163], v[44:47]\n\t" \
  "v_mfma_f32_16x16x32_bf16 v[64:67], v[104:107], v[164:167], v[64:67]\n\t" \
  "v_mfma_f32_16x16x32_bf16 v[84:87], v[104:107], v[168:171], v[84:87]\n\t" \
  "v_mfma_f32_16x16x32_bf16 v[28:31], v[108:111], v[156:159], v[28:31]\n\t" \
  "v_mfma_f32_16x16x32_bf16 v[48:51], v[108:111], v[160:163], v[48:51]\n\t" \
  "v_mfma_f32_16x16x32_bf16 v[68:71], v[108:111], v[164:167], v[68:71]\n\t" \
  "v_mfma_f32_16x16x32_bf16 v[88:91], v[108:111], v[168:171], v[88:91]\n\t" \
  "v_mfma_f32_16x16x32_bf16 v[32:35], v[112:115], v[156:159], v[32:35]\n\t" \
  "v_mfma_f32_16x16x32_bf16 v[52:55], v[112:115], v[160:163], v[52:55]\n\t" \
  "v_mfma_f32_16x16x32_bf16 v[72:75], v[112:115], v[164:167], v[72:75]\n\t" \
  "v_mfma_f32_16x16x32_bf16 v[92:95], v[112:115], v[168:171], v[92:95]\n\t" \
  "global_load_dwordx4 v[96:99], v240, s[60:61] sc1\n\t" \
  "global_load_dwordx4 v[100:103], v240, s[60:61] offset:1024 sc1\n\t" \
  "global_load_dwordx4 v[104:107], v240, s[60:61] offset:2048 sc1\n\t" \
  "global_load_dwordx4 v[108:111], v240, s[60:61] offset:3072 sc1\n\t" \
  "global_load_dwordx4 v[112:115], v245, s[60:61] sc1\n\t" \
  "s_add_u32 s60, s60, 5120\n\t" \
  "s_addc_u32 s61, s61, 0\n\t" \
  "v_mfma_f32_16x16x32_bf16 v[16:19], v[116:119], v[172:175], v[16:19]\n\t" \
  "v_mfma_f32_16x16x32_bf16 v[36:39], v[116:119], v[176:179], v[36:39]\n\t" \
  "v_mfma_f32_16x16x32_bf16 v[56:59], v[116:119], v[180:183], v[56:59]\n\t" \
  "v_mfma_f32_16x16x32_bf16 v[76:79], v[116:119], v[184:187], v[76:79]\n\t" \
  "v_mfma_f32_16x16x32_bf16 v[20:23], v[120:123], v[172:175], v[20:23]\n\t" \
  "v_mfma_f32_16x16x32_bf16 v[40:43], v[120:123], v[176:179], v[40:43]\n\t" \
  "v_mfma_f32_16x16x32_bf16 v[60:63], v[120:123], v[180:183], v[60:63]\n\t" \
  "v_mfma_f32_16x16x32_bf16 v[80:83], v[120:123], v[184:187], v[80:83]\n\t" \
  "v_mfma_f32_16x16x32_bf16 v[24:27], v[124:127], v[172:175], v[24:27]\n\t" \
  "v_mfma_f32_16x16x32_bf16 v[44:47], v[124:127], v[176:179], v[44:47]\n\t" \
  "v_mfma_f32_16x16x32_bf16 v[64:67], v[124:127], v[180:183], v[64:67]\n\t" \
  "v_mfma_f32_16x16x32_bf16 v[84:87], v[124:127], v[184:187], v[84:87]\n\t" \
  "v_mfma_f32_16x16x32_bf16 v[28:31], v[128:131], v[172:175], v[28:31]\n\t" \
  "v_mfma_f32_16x16x32_bf16 v[48:51], v[128:131], v[176:179], v[48:51]\n\t" \
  "v_mfma_f32_16x16x32_bf16 v[68:71], v[128:131], v[180:183], v[68:71]\n\t" \
  "v_mfma_f32_16x16x32_bf16 v[88:91], v[128:131], v[184:187], v[88:91]\n\t" \
  "v_mfma_f32_16x16x32_bf16 v[32:35], v[132:135], v[172:175], v[32:35]\n\t" \
  "v_mfma_f32_16x16x32_bf16 v[52:55], v[132:135], v[176:179], v[52:55]\n\t" \
  "v_mfma_f32_16x16x32_bf16 v[72:75], v[132:135], v[180:183], v[72:75]\n\t" \
  "v_mfma_f32_16x16x32_bf16 v[92:95], v[132:135], v[184:187], v[92:95]\n\t" \
  "global_load_dwordx4 v[116:119], v240, s[60:61] sc1\n\t" \
  "global_load_dwordx4 v[120:123], v240, s[60:61] offset:1024 sc1\n\t" \
  "global_load_dwordx4 v[124:127], v240, s[60:61] offset:2048 sc1\n\t" \
  "global_load_dwordx4 v[128:131], v240, s[60:61] offset:3072 sc1\n\t" \
  "global_load_dwordx4 v[132:135], v245, s[60:61] sc1\n\t" \
  "s_add_u32 s60, s60, 5120\n\t" \
  "s_addc_u32 s61, s61, 0\n\t" \
  "v_mfma_f32_16x16x32_bf16 v[16:19], v[136:139], v[188:191], v[16:19]\n\t" \
  "v_mfma_f32_16x16x32_bf16 v[36:39], v[136:139], v[192:195], v[36:39]\n\t" \
  "v_mfma_f32_16x16x32_bf16 v[56:59], v[136:139], v[196:199], v[56:59]\n\t" \
  "v_mfma_f32_16x16x32_bf16 v[76:79], v[136:139], v[200:203], v[76:79]\n\t" \
  "v_mfma_f32_16x16x32_bf16 v[20:23], v[140:143], v[188:191], v[20:23]\n\t" \
  "v_mfma_f32_16x16x32_bf16 v[40:43], v[140:143], v[192:195], v[40:43]\n\t" \
  "v_mfma_f32_16x16x32_bf16 v[60:63], v[140:143], v[196:199], v[60:63]\n\t" \
  "v_mfma_f32_16x16x32_bf16 v[80:83], v[140:143], v[200:203], v[80:83]\n\t" \
  "v_mfma_f32_16x16x32_bf16 v[24:27], v[144:147], v[188:191], v[24:27]\n\t" \
  "v_mfma_f32_16x16x32_bf16 v[44:47], v[144:147], v[192:195], v[44:47]\n\t" \
  "v_mfma_f32_16x16x32_bf16 v[64:67], v[144:147], v[196:199], v[64:67]\n\t" \
  "v_mfma_f32_16x16x32_bf16 v[84:87], v[144:147], v[200:203], v[84:87]\n\t" \
  "v_mfma_f32_16x16x32_bf16 v[28:31], v[148:151], v[188:191], v[28:31]\n\t" \
  "v_mfma_f32_16x16x32_bf16 v[48:51], v[148:151], v[192:195], v[48:51]\n\t" \
  "v_mfma_f32_16x16x32_bf16 v[68:71], v[148:151], v[196:199], v[68:71]\n\t" \
  "v_mfma_f32_16x16x32_bf16 v[88:91], v[148:151], v[200:203], v[88:91]\n\t" \
  "v_mfma_f32_16x16x32_bf16 v[32:35], v[152:155], v[188:191], v[32:35]\n\t" \
  "v_mfma_f32_16x16x32_bf16 v[52:55], v[152:155], v[192:195], v[52:55]\n\t" \
  "v_mfma_f32_16x16x32_bf16 v[72:75], v[152:155], v[196:199], v[72:75]\n\t" \
  "v_mfma_f32_16x16x32_bf16 v[92:95], v[152:155], v[200:203], v[92:95]\n\t" \
  "s_waitcnt vmcnt(5)\n\t" \
  "v_mfma_f32_16x16x32_bf16 v[16:19], v[96:99], v[204:207], v[16:19]\n\t" \
  "v_mfma_f32_16x16x32_bf16 v[36:39], v[96:99], v[208:211], v[36:39]\n\t" \
  "v_mfma_f32_16x16x32_bf16 v[56:59], v[96:99], v[212:215], v[56:59]\n\t" \
  "v_mfma_f32_16x16x32_bf16 v[76:79], v[96:99], v[216:219], v[76:79]\n\t" \
  "v_mfma_f32_16x16x32_bf16 v[20:23], v[100:103], v[204:207], v[20:23]\n\t" \
  "v_mfma_f32_16x16x32_bf16 v[40:43], v[100:103], v[208:211], v[40:43]\n\t" \
  "v_mfma_f32_16x16x32_bf16 v[60:63], v[100:103], v[212:215], v[60:63]\n\t" \
  "v_mfma_f32_16x16x32_bf16 v[80:83], v[100:103], v[216:219], v[80:83]\n\t" \
  "v_mfma_f32_16x16x32_bf16 v[24:27], v[104:107], v[204:207], v[24:27]\n\t" \
  "v_mfma_f32_16x16x32_bf16 v[44:47], v[104:107], v[208:211], v[44:47]\n\t" \
  "v_mfma_f32_16x16x32_bf16 v[64:67], v[104:107], v[212:215], v[64:67]\n\t" \
  "v_mfma_f32_16x16x32_bf16 v[84:87], v[104:107], v[216:219], v[84:87]\n\t" \
  "v_mfma_f32_16x16x32_bf16 v[28:31], v[108:111], v[204:207], v[28:31]\n\t" \
  "v_mfma_f32_16x16x32_bf16 v[48:51], v[108:111], v[208:211], v[48:51]\n\t" \
  "v_mfma_f32_16x16x32_bf16 v[68:71], v[108:111], v[212:215], v[68:71]\n\t" \
  "v_mfma_f32_16x16x32_bf16 v[88:91], v[108:111], v[216:219], v[88:91]\n\t" \
  "v_mfma_f32_16x16x32_bf16 v[32:35], v[112:115], v[204:207], v[32:35]\n\t" \
  "v_mfma_f32_16x16x32_bf16 v[52:55], v[112:115], v[208:211], v[52:55]\n\t" \
  "v_mfma_f32_16x16x32_bf16 v[72:75], v[112:115], v[212:215], v[72:75]\n\t" \
  "v_mfma_f32_16x16x32_bf16 v[92:95], v[112:115], v[216:219], v[92:95]\n\t" \
  "s_waitcnt vmcnt(0)\n\t" \
  "v_mfma_f32_16x16x32_bf16 v[16:19], v[116:119], v[220:223], v[16:19]\n\t" \
  "v_mfma_f32_16x16x32_bf16 v[36:39], v[116:119], v[224:227], v[36:39]\n\t" \
  "v_mfma_f32_16x16x32_bf16 v[56:59], v[116:119], v[228:231], v[56:59]\n\t" \
  "v_mfma_f32_16x16x32_bf16 v[76:79], v[116:119], v[232:235], v[76:79]\n\t" \
  "v_mfma_f32_16x16x32_bf16 v[20:23], v[120:123], v[220:223], v[20:23]\n\t" \
  "v_mfma_f32_16x16x32_bf16 v[40:43], v[120:123], v[224:227], v[40:43]\n\t" \
  "v_mfma_f32_16x16x32_bf16 v[60:63], v[120:123], v[228:231], v[60:63]\n\t" \
  "v_mfma_f32_16x16x32_bf16 v[80:83], v[120:123], v[232:235], v[80:83]\n\t" \
  "v_mfma_f32_16x16x32_bf16 v[24:27], v[124:127], v[220:223], v[24:27]\n\t" \
  "v_mfma_f32_16x16x32_bf16 v[44:47], v[124:127], v[224:227], v[44:47]\n\t" \
  "v_mfma_f32_16x16x32_bf16 v[64:67], v[124:127], v[228:231], v[64:67]\n\t" \
  "v_mfma_f32_16x16x32_bf16 v[84:87], v[124:127], v[232:235], v[84:87]\n\t" \
  "v_mfma_f32_16x16x32_bf16 v[28:31], v[128:131], v[220:223], v[28:31]\n\t" \
  "v_mfma_f32_16x16x32_bf16 v[48:51], v[128:131], v[224:227], v[48:51]\n\t" \
  "v_mfma_f32_16x16x32_bf16 v[68:71], v[128:131], v[228:231], v[68:71]\n\t" \
  "v_mfma_f32_16x16x32_bf16 v[88:91], v[128:131], v[232:235], v[88:91]\n\t" \
  "v_mfma_f32_16x16x32_bf16 v[32:35], v[132:135], v[220:223], v[32:35]\n\t" \
  "v_mfma_f32_16x16x32_bf16 v[52:55], v[132:135], v[224:227], v[52:55]\n\t" \
  "v_mfma_f32_16x16x32_bf16 v[72:75], v[132:135], v[228:231], v[72:75]\n\t" \
  "v_mfma_f32_16x16x32_bf16 v[92:95], v[132:135], v[232:235], v[92:95]\n\t" \
  "s_nop 15\n\t" \
  ""
#define DOWN_CLOB "v96","v97","v98","v99",V10(10),V10(11),V10(12),V10(13),V10(14),V10(15),V10(16),V10(17),V10(18),V10(19),V10(20),V10(21),V10(22),V10(23),"scc","memory"
#define WO_CLOB "v12","v13","v96","v97","v98","v99",V10(10),V10(11),V10(12),V10(13),V10(14),V10(15),V10(16),V10(17),V10(18),V10(19),V10(20),V10(21),V10(22),"v230","v231","v232","v233","v234","v235", \
    "s64","s70","s71","s72","s73","vcc","scc","memory"
DI void role_out(char* ws, uint16_t* out, int b, unsigned tag, char* smem) {
    const int tt = (b & 7) * 4 + (b >> 6), ng = (b >> 3) & 7, tid = threadIdx.x, w = rfl(tid >> 6);
    int lane = tid & 63;
    if (w == 0) { const unsigned offs[1] = {lane < 14 ? WS_HF + (tt >> 1) * 128 + lane * 8u : lane < 18 ? WS_KVF + tt * 32 + (lane - 14) * 8u : WS_RF + min(lane - 18, 31) * 8u}; poll<1>(ws, offs, tag); }
    LBAR();
    { const unsigned long long t0 = __builtin_amdgcn_s_memrealtime(); while (__builtin_amdgcn_s_memrealtime() - t0 < DNDELAY) __builtin_amdgcn_s_sleep(2); }
    f32x16 acc[5];
#pragma unroll
    for (int a = 0; a < 5; ++a) acc[a] = f32x16{};
    {
        const int k0 = w < 2 ? 14 * w : 28 + 13 * (w - 2);
        const unsigned va = lane * 16;
        uint64_t ap = uni64(ws + WS_WT + ((size_t)ng * 74 + k0) * 5120), hp = uni64(ws + WS_H + ((size_t)tt * 4 * 54 + k0) * 1024);
        asm volatile(DOWN3_ASM
            : "+{v[16:31]}"(acc[0]), "+{v[32:47]}"(acc[1]), "+{v[48:63]}"(acc[2]), "+{v[64:79]}"(acc[3]), "+{v[80:95]}"(acc[4]), "+{s[60:61]}"(ap), "+{s[62:63]}"(hp)
            : "{v240}"(va), "{v245}"(va + 4096), "{v241}"(va), "{v242}"(va + 54 * 1024), "{v243}"(va + 2 * 54 * 1024), "{v244}"(va + 3 * 54 * 1024), "{s69}"(rfl(w < 2 ? 1u : 0u)), [pl] "n"(GP_DN)
            : DOWN_CLOB);
    }
    PRIO(GP_HI);
    lane = fresh_lane();
    const rsrc_t R = ws_rsrc(ws);
#pragma unroll
    for (int tb = 0; tb < 4; ++tb) {
        const float omg = bf16r(1.f - __uint_as_float(ld4(R, WS_G + (tt * 64 + tb * 16 + (lane & 15)) * 4)));
#pragma unroll
        for (int e = 0; e < 20; ++e) acc[(20 * tb + e) >> 4][(20 * tb + e) & 15] *= omg;
    }
    { const unsigned offs[1] = {WS_AW + (tt * 32 + (lane & 31)) * 8u}; poll<1>(ws, offs, tag); }
    {
        const unsigned va = lane * 16;
        uint64_t ap = uni64(ws + WS_WT + ((size_t)ng * 74 + 54 + 5 * w) * 5120), op = uni64(ws + WS_O + ((size_t)tt * 4 * 20 + 5 * w) * 1024), fp = uni64(ws + WS_O + ((size_t)tt * 4 * 20 + 5 * w + 4) * 1024);
        asm volatile(WO3_ASM
            : "+{v[16:31]}"(acc[0]), "+{v[32:47]}"(acc[1]), "+{v[48:63]}"(acc[2]), "+{v[64:79]}"(acc[3]), "+{v[80:95]}"(acc[4]), "+{s[60:61]}"(ap), "+{s[62:63]}"(op)
            : "{v240}"(va), "{v245}"(va + 4096), "{v241}"(va), "{v242}"(va + 20 * 1024), "{v243}"(va + 2 * 20 * 1024), "{v244}"(va + 3 * 20 * 1024), "{s[66:67]}"(fp)
            : WO_CLOB);
    }
    lane = fresh_lane();
    lds_ptr Rd = (lds_ptr)smem + lane * 16;
#pragma unroll
    for (int i = 0; i < 20; ++i) *(LAS f32x4*)(Rd + (w * 20 + i) * 1024) = f32x4{acc[i >> 2][4 * (i & 3)], acc[i >> 2][4 * (i & 3) + 1], acc[i >> 2][4 * (i & 3) + 2], acc[i >> 2][4 * (i & 3) + 3]};
    LBAR();
    f32x4 part[5][4];
#pragma unroll
    for (int c = 0; c < 5; ++c)
#pragma unroll
        for (int s = 0; s < 4; ++s) part[c][s] = *(LAS f32x4*)(Rd + (s * 20 + w * 5 + c) * 1024);
    uint16_t* orow_ = out + (size_t)(tt * 64 + w * 16 + (lane & 15)) * D + ng * 80 + 4 * (lane >> 4);
#pragma unroll
    for (int c = 0; c < 5; ++c) { const f32x4 v = (part[c][0] + part[c][1]) + (part[c][2] + part[c][3]); *(u32x2*)(orow_ + 16 * c) = u32x2{cvtpk(v[0], v[1]), cvtpk(v[2], v[3])}; }
}

__global__ void __launch_bounds__(256, 2) k_fused(const uint16_t* x, const uint16_t* wq, const uint16_t* wk, const uint16_t* wv, const uint16_t* wo, const uint16_t* wg, const uint16_t* wu,
                                                  const uint16_t* wd, const uint16_t* gw, char* ws, uint16_t* out) {
    extern __shared__ __attribute__((aligned(16))) char smem[];
    const int b = blockIdx.x;
    if (b >= 256) { role_attn(x, wq, ws, b - 256, smem); return; }
    const unsigned ep = __hip_atomic_load((GAS unsigned*)(ws + WS_EPOCH), RLX_AGENT);
    const int g = b, w = rfl(threadIdx.x >> 6), lane = threadIdx.x & 63;
    const int xj = g & 7, idx = g >> 3;
    PRIO(3);
    unsigned tag;
    if (idx < 28) {
        const int pp = (xj >> 1) * 4 + idx / 7, cg = (xj & 1) * 7 + idx % 7;
        if (idx % 7 < 4) { const int d = (idx / 7) * 4 + idx % 7; kv_duty(x, (d & 1) ? wv : wk, gw, ws, 16 * (xj >> 1) + 8 * (xj & 1) + (d >> 1), d & 1, ep, smem); }
        PRIO(GP_UP);
        ffn_up(x, wg, wu, ws, pp, cg, smem);
        tag = make_tag(ep);
        publish(ws, WS_HF + (pp * 16 + cg) * 8, tag);
    } else {
        PRIO(GP_RT);
        {
            const int wv_ = (xj * 4 + idx - 28) * 4 + w; const unsigned dst = (unsigned)(uintptr_t)smem + w * 1024;
            for (int i = 0; i < 17; ++i) { const int pc = min(i * 128 + wv_, 2159); glds_s((const char*)wg + (size_t)pc * 1024, lane * 16, dst); glds_s((const char*)wu + (size_t)pc * 1024, lane * 16, dst); }
            WAITBAR(0);
        }
        retile(wd, wo, ws, xj * 4 + idx - 28, smem);
        tag = make_tag(ep);
        publish(ws, WS_RF + (xj * 4 + idx - 28) * 8, tag);
    }
    if (g == 0 && w == 3) {
        unsigned offs[8];
#pragma unroll
        for (int k = 0; k < 4; ++k) { const int i = min(lane * 4 + k, 223); offs[k] = WS_HF + ((i / 14) * 16 + i % 14) * 8; offs[4 + k] = WS_AW + (lane * 4 + k) * 32; }
        poll<8>(ws, offs, tag);
        const unsigned o2[1] = {WS_RF + (lane & 31) * 8u}; poll<1>(ws, o2, tag);
        if (lane == 0) __hip_atomic_store((GAS unsigned*)(ws + WS_EPOCH), tag, RLX_AGENT);
    }
    role_out(ws, out, g, tag, smem);
}

extern "C" void kernel_launch(void* const* d_in, const int* in_sizes, int n_in, void* d_out, int out_size, void* d_ws, size_t ws_size, hipStream_t stream) {
    const uint16_t* x = (const uint16_t*)d_in[0]; const uint16_t* wq = (const uint16_t*)d_in[1]; const uint16_t* wk = (const uint16_t*)d_in[2];
    const uint16_t* wv = (const uint16_t*)d_in[3]; const uint16_t* wo = (const uint16_t*)d_in[4]; const uint16_t* wg = (const uint16_t*)d_in[6];
    const uint16_t* wu = (const uint16_t*)d_in[7]; const uint16_t* wd = (const uint16_t*)d_in[8]; const uint16_t* gw = (const uint16_t*)d_in[9];
    (void)hipFuncSetAttribute((const void*)k_fused, hipFuncAttributeMaxDynamicSharedMemorySize, LDS_BYTES);
    k_fused<<<512, 256, LDS_BYTES, stream>>>(x, wq, wk, wv, wo, wg, wu, wd, gw, (char*)d_ws, (uint16_t*)d_out);
}
```
